```python
import torch

def _softmax(x, axis=-1):
    return torch.softmax(x, dim=axis)
def _gelu(x):
    return torch.nn.functional.gelu(x, approximate="tanh")
def _silu(x):
    return torch.nn.functional.silu(x)
def _sigmoid(x):
    return torch.sigmoid(x)
def _layernorm(x, axis=-1, eps=1e-5):
    m = x.mean(dim=axis, keepdim=True)
    v = x.var(dim=axis, keepdim=True, unbiased=False)
    return (x - m) / torch.sqrt(v + eps)
def _scatter_add(tgt, idx, src, axis=0):
    return tgt.clone().index_add_(axis, idx, src)
def _segment_sum(src, seg, n):
    out = torch.zeros((n,) + src.shape[1:], dtype=src.dtype)
    return out.index_add_(0, seg, src)
def _pad(x, pw):
    flat = tuple(v for pair in reversed(pw) for v in pair)
    return torch.nn.functional.pad(x, flat)

kernel_name = 'standalone__gqa_block_cached__sampling'

def setup_inputs():

    _w = (lambda a: (lambda t: t.to(torch.bfloat16) if t.is_floating_point() else t)(torch.as_tensor(a)))
    B, Sq, D = 32, 4, 576
    Hq, Hkv, Sk, Dh = 32, 16, 2048, 128
    assert Hq % Hkv == 0, (Hq, Hkv)
    return {
        "x": _w((lambda *s: torch.randn(*s, dtype=torch.bfloat16) * 0.8)(B, Sq, D)),
        "k_cache": _w((lambda *s: torch.randn(*s, dtype=torch.bfloat16) * 0.8)(B, Hkv, Sk, Dh)),
        "v_cache": _w((lambda *s: torch.randn(*s, dtype=torch.bfloat16) * 0.05)(B, Hkv, Sk, Dh)),
        "wq": _w((lambda *s: torch.randn(*s, dtype=torch.bfloat16) * 0.8)(D, Hq * Dh) * (D ** -0.5)),
        "wo": _w((lambda *s: torch.randn(*s, dtype=torch.bfloat16) * 0.05)(Hq * Dh, D) * ((Hq * Dh) ** -0.5)),
    }

def reference(x, k_cache, v_cache, wq, wo):
    B, Sq, D = x.shape
    Hq, Dh = 32, 128
    Hkv = k_cache.shape[1]
    rep = Hq // Hkv
    q = (lambda x, *d: x.permute(*d))((x @ wq).reshape(B, Sq, Hq, Dh), 0, 2, 1, 3)
    k = (lambda x, n, axis: torch.repeat_interleave(x, n, dim=axis))(k_cache, rep, axis=1)
    v = (lambda x, n, axis: torch.repeat_interleave(x, n, dim=axis))(v_cache, rep, axis=1)
    scores = torch.einsum("bhqd,bhkd->bhqk", q, k) * (Dh ** -0.5)
    p = _softmax(scores, axis=-1)
    o = torch.einsum("bhqk,bhkd->bhqd", p, v)
    return (lambda x, *d: x.permute(*d))(o, 0, 2, 1, 3).reshape(B, Sq, Hq * Dh) @ wo
```

```cpp
#include <hip/hip_runtime.h>
#include <hip/hip_bf16.h>

#define POLICY 0

namespace {

constexpr int NB = 32, SQ = 4, DM = 576, HQ = 32, HKV = 16, SK = 2048, DH = 128;
constexpr int NWG = 256, NTHR = 512;
constexpr unsigned KV_BYTES = (unsigned)NB * HKV * SK * DH * 2u;
constexpr unsigned X_BYTES = NB * SQ * DM * 2, WQ_BYTES = DM * HQ * DH * 2, WO_BYTES = HQ * DH * DM * 2;
constexpr int TILES = 16;
constexpr int LDS_QP = 131072;
constexpr int LDS_BYTES = 163840;
constexpr int OUT_ROW = 2320;
constexpr int LDS_OUT = 0, LDS_REM = 8 * OUT_ROW;
constexpr float SC = 0.08838834764831845f * 1.4426950408889634f;

typedef unsigned u32x4 __attribute__((ext_vector_type(4)));
typedef unsigned u32x2 __attribute__((ext_vector_type(2)));
typedef float f32x4 __attribute__((ext_vector_type(4)));
typedef float f32x2 __attribute__((ext_vector_type(2)));
typedef __bf16 bf16x8 __attribute__((ext_vector_type(8)));
typedef __bf16 bf16x2 __attribute__((ext_vector_type(2)));
#define LAS __attribute__((address_space(3)))
#define DI __device__ __forceinline__

DI unsigned rfl(unsigned x) { return __builtin_amdgcn_readfirstlane(x); }
DI unsigned cvtpk(float lo, float hi) { f32x2 v = {lo, hi}; bf16x2 b = __builtin_convertvector(v, bf16x2); return __builtin_bit_cast(unsigned, b); }
DI f32x4 mfma(u32x4 a, u32x4 b, f32x4 c) { return __builtin_amdgcn_mfma_f32_16x16x32_bf16(__builtin_bit_cast(bf16x8, a), __builtin_bit_cast(bf16x8, b), c, 0, 0, 0); }
DI unsigned perm_lo(unsigned second, unsigned first) { return __builtin_amdgcn_perm(second, first, 0x05040100u); }
DI unsigned perm_hi(unsigned second, unsigned first) { return __builtin_amdgcn_perm(second, first, 0x07060302u); }
template <int N> DI void waitvm() { asm volatile("s_waitcnt vmcnt(%0)" ::"n"(N) : "memory"); }
DI void waitlgkm0() { asm volatile("s_waitcnt lgkmcnt(0)" ::: "memory"); }
DI void barrier() { asm volatile("s_waitcnt lgkmcnt(0)\n\ts_barrier" ::: "memory"); }
DI float xmax16_32(float x) {
  auto s = __builtin_amdgcn_permlane16_swap(__float_as_uint(x), __float_as_uint(x), false, false);
  x = fmaxf(__uint_as_float(s[0]), __uint_as_float(s[1]));
  auto t = __builtin_amdgcn_permlane32_swap(__float_as_uint(x), __float_as_uint(x), false, false);
  return fmaxf(__uint_as_float(t[0]), __uint_as_float(t[1]));
}
DI float xsum16_32(float x) {
  auto s = __builtin_amdgcn_permlane16_swap(__float_as_uint(x), __float_as_uint(x), false, false);
  x = __uint_as_float(s[0]) + __uint_as_float(s[1]);
  auto t = __builtin_amdgcn_permlane32_swap(__float_as_uint(x), __float_as_uint(x), false, false);
  return __uint_as_float(t[0]) + __uint_as_float(t[1]);
}
DI void dma4(unsigned m0, unsigned v0, unsigned v1, unsigned v2, unsigned v3, __amdgpu_buffer_rsrc_t srd, unsigned soff) {
  asm volatile("s_nop 2\n\ts_mov_b32 m0, %0\n\ts_nop 0\n\t"
               "buffer_load_dwordx4 %1, %5, %6 offen nt lds\n\t"
               "buffer_load_dwordx4 %2, %5, %6 offen offset:1024 nt lds\n\t"
               "buffer_load_dwordx4 %3, %5, %6 offen offset:2048 nt lds\n\t"
               "buffer_load_dwordx4 %4, %5, %6 offen offset:3072 nt lds"
               ::"s"(m0), "v"(v0), "v"(v1), "v"(v2), "v"(v3), "s"(srd), "s"(soff) : "memory");
}
template <class T> DI T lds_ld(unsigned a) { return *(const LAS T*)(size_t)a; }
template <class T> DI void lds_st(unsigned a, T v) { *(LAS T*)(size_t)a = v; }

__global__ __launch_bounds__(NTHR, 2) void attn_fused_kernel(const void* __restrict__ xg, const void* __restrict__ kg, const void* __restrict__ vg,
                                                            const void* __restrict__ wqg, const void* __restrict__ wog, float* __restrict__ slab) {
  extern __shared__ __attribute__((aligned(16))) char lds_raw[];
  const unsigned lds0 = (unsigned)(size_t)lds_raw;
  const unsigned tid = threadIdx.x, lane = tid & 63, wave = rfl(tid >> 6);
  const unsigned l15 = lane & 15, g = lane >> 4;
  const unsigned bx = blockIdx.x & 7, bi = blockIdx.x >> 3;
  const unsigned kvh = 2 * bx + (bi & 1), bp = bi >> 1;
  const unsigned bb = wave >> 2, wq4 = wave & 3;
  const unsigned batch = 2 * bp + bb;

  const __amdgpu_buffer_rsrc_t srdK = __builtin_amdgcn_make_buffer_rsrc((void*)kg, (short)0, (int)KV_BYTES, 0x00020000);
  const __amdgpu_buffer_rsrc_t srdV = __builtin_amdgcn_make_buffer_rsrc((void*)vg, (short)0, (int)KV_BYTES, 0x00020000);
  const __amdgpu_buffer_rsrc_t srdX = __builtin_amdgcn_make_buffer_rsrc((void*)xg, (short)0, (int)X_BYTES, 0x00020000);
  const __amdgpu_buffer_rsrc_t srdWQ = __builtin_amdgcn_make_buffer_rsrc((void*)wqg, (short)0, (int)WQ_BYTES, 0x00020000);
  const __amdgpu_buffer_rsrc_t srdWO = __builtin_amdgcn_make_buffer_rsrc((void*)wog, (short)0, (int)WO_BYTES, 0x00020000);

  const unsigned kslot = lds0 + wave * 16384u, vslot = kslot + 8192u;
  const unsigned tile0 = (batch * HKV + kvh) * (unsigned)(SK * DH * 2) + wq4 * 8192u;
  unsigned kv[4];
#pragma unroll
  for (int i = 0; i < 4; ++i) kv[i] = g * 256u + ((l15 ^ (4u * i + g)) * 16u);
  const unsigned vv = lane * 16u;
  auto issueK = [&](unsigned n) {
    const unsigned so = tile0 + n * 32768u;
    waitlgkm0();
    dma4(kslot, kv[0], kv[1], kv[2], kv[3], srdK, so);
    dma4(kslot + 4096u, kv[0], kv[1], kv[2], kv[3], srdK, so + 4096u);
  };
  auto issueV = [&](unsigned n) {
    const unsigned so = tile0 + n * 32768u;
    waitlgkm0();
    dma4(vslot, vv, vv, vv, vv, srdV, so);
    dma4(vslot + 4096u, vv, vv, vv, vv, srdV, so + 4096u);
  };

  issueK(0);
  __builtin_amdgcn_sched_barrier(0);

  u32x4 qf[4];
  {
    const unsigned nh = wave & 1, ig = wave >> 1;
    const unsigned ib0 = ig < 2 ? 5 * ig : 10 + 4 * (ig - 2);
    const bool five = ig < 2;
    const unsigned xoff = (8 * bp + (lane & 7)) * (DM * 2) + g * 16u;
    const unsigned woff = g * (8u * 8192u) + l15 * 16u;
    const unsigned wso = ib0 * (32u * 8192u) + kvh * 512u + nh * 256u;
    u32x4 xf[5], w[5][8];
#pragma unroll
    for (int k = 0; k < 4; ++k) {
      xf[k] = __builtin_amdgcn_raw_buffer_load_b128(srdX, xoff + k * 64, ib0 * 64u, 0);
#pragma unroll
      for (int j = 0; j < 8; ++j) w[k][j] = __builtin_amdgcn_raw_buffer_load_b128(srdWQ, woff + j * 8192u, wso + k * (32u * 8192u), 0);
    }
    if (five) {
      xf[4] = __builtin_amdgcn_raw_buffer_load_b128(srdX, xoff + 4 * 64, ib0 * 64u, 0);
#pragma unroll
      for (int j = 0; j < 8; ++j) w[4][j] = __builtin_amdgcn_raw_buffer_load_b128(srdWQ, woff + j * 8192u, wso + 4 * (32u * 8192u), 0);
    } else {
      xf[4] = u32x4{0, 0, 0, 0};
#pragma unroll
      for (int j = 0; j < 8; ++j) w[4][j] = u32x4{0, 0, 0, 0};
    }
    __builtin_amdgcn_sched_barrier(0);
    issueV(0);
    __builtin_amdgcn_sched_barrier(0);
    f32x4 qa[8];
#pragma unroll
    for (int d = 0; d < 8; ++d) qa[d] = f32x4{0.f, 0.f, 0.f, 0.f};
#pragma unroll
    for (int k = 0; k < 5; ++k) {
#pragma unroll
      for (int d = 0; d < 8; ++d) {
        u32x4 a;
#pragma unroll
        for (int u = 0; u < 4; ++u) a[u] = (d & 1) ? perm_hi(w[k][2 * u + 1][d >> 1], w[k][2 * u][d >> 1]) : perm_lo(w[k][2 * u + 1][d >> 1], w[k][2 * u][d >> 1]);
        qa[d] = mfma(a, xf[k], qa[d]);
      }
    }
    if (l15 < 8) {
      const unsigned base = lds0 + LDS_QP + (ig * 2 + nh) * 4096u + l15 * 512u, key = l15 ^ (nh << 2);
#pragma unroll
      for (int r = 0; r < 4; ++r) {
        lds_st<f32x4>(base + (((8 * g + 2 * r) ^ key) * 16u), f32x4{qa[0][r], qa[1][r], qa[2][r], qa[3][r]});
        lds_st<f32x4>(base + (((8 * g + 2 * r + 1) ^ key) * 16u), f32x4{qa[4][r], qa[5][r], qa[6][r], qa[7][r]});
      }
    }
    barrier();
    const unsigned q = lane & 7, qh = q >> 2, m = bb * 4 + (q & 3), key = m ^ (qh << 2);
    const unsigned rb = lds0 + LDS_QP + qh * 4096u + m * 512u;
#pragma unroll
    for (int st = 0; st < 4; ++st) {
      f32x4 a = {0.f, 0.f, 0.f, 0.f}, b = {0.f, 0.f, 0.f, 0.f};
#pragma unroll
      for (int i = 0; i < 4; ++i) {
        a += lds_ld<f32x4>(rb + i * 8192u + (((8 * st + 2 * g) ^ key) * 16u));
        b += lds_ld<f32x4>(rb + i * 8192u + (((8 * st + 2 * g + 1) ^ key) * 16u));
      }
      qf[st] = u32x4{cvtpk(a[0], a[1]), cvtpk(a[2], a[3]), cvtpk(b[0], b[1]), cvtpk(b[2], b[3])};
    }
  }

  f32x4 acc[8];
#pragma unroll
  for (int d = 0; d < 8; ++d) acc[d] = f32x4{0.f, 0.f, 0.f, 0.f};
  float mrun = -INFINITY, lrun = 0.f;
  const unsigned kra = kslot + l15 * 256u + ((g ^ l15) * 16u);
  const unsigned vra = vslot + g * 1024u + l15 * 16u;

  for (int n = 0; n < TILES; ++n) {
    if (POLICY == 0) { waitvm<8>(); } else { if (n == 0) waitvm<8>(); else { waitvm<0>(); issueV(n); } }
    u32x4 kf[2][4];
#pragma unroll
    for (int T = 0; T < 2; ++T)
#pragma unroll
      for (int st = 0; st < 4; ++st) kf[T][st] = lds_ld<u32x4>((kra ^ (st << 6)) + T * 4096u);
    if (POLICY == 0) { if (n + 1 < TILES) issueK(n + 1); }
    f32x4 s0 = {0.f, 0.f, 0.f, 0.f}, s1 = {0.f, 0.f, 0.f, 0.f};
#pragma unroll
    for (int st = 0; st < 4; ++st) { s0 = mfma(kf[0][st], qf[st], s0); s1 = mfma(kf[1][st], qf[st], s1); }
    float mx = fmaxf(fmaxf(fmaxf(s0[0], s0[1]), fmaxf(s0[2], s0[3])), fmaxf(fmaxf(s1[0], s1[1]), fmaxf(s1[2], s1[3])));
    mx = xmax16_32(mx);
    const float mnew = fmaxf(mrun, mx);
    const float alpha = __builtin_amdgcn_exp2f((mrun - mnew) * SC);
    const float nm = -mnew * SC;
    mrun = mnew;
    float p[8];
#pragma unroll
    for (int r = 0; r < 4; ++r) { p[r] = __builtin_amdgcn_exp2f(fmaf(s0[r], SC, nm)); p[4 + r] = __builtin_amdgcn_exp2f(fmaf(s1[r], SC, nm)); }
    lrun = fmaf(lrun, alpha, ((p[0] + p[1]) + (p[2] + p[3])) + ((p[4] + p[5]) + (p[6] + p[7])));
    const u32x4 pf = {cvtpk(p[0], p[1]), cvtpk(p[2], p[3]), cvtpk(p[4], p[5]), cvtpk(p[6], p[7])};
#pragma unroll
    for (int d = 0; d < 8; ++d) acc[d] *= alpha;
    if (POLICY == 0) { if (n + 1 < TILES) waitvm<8>(); else waitvm<0>(); }
    else { waitvm<0>(); if (n + 1 < TILES) issueK(n + 1); }
    u32x4 vr[8];
#pragma unroll
    for (int j = 0; j < 8; ++j) vr[j] = lds_ld<u32x4>(vra + (j >> 2) * 4096u + (j & 3) * 256u);
    if (POLICY == 0) { if (n + 1 < TILES) issueV(n + 1); }
#pragma unroll
    for (int d = 0; d < 8; ++d) {
      u32x4 a;
#pragma unroll
      for (int u = 0; u < 4; ++u) a[u] = (d & 1) ? perm_hi(vr[2 * u + 1][d >> 1], vr[2 * u][d >> 1]) : perm_lo(vr[2 * u + 1][d >> 1], vr[2 * u][d >> 1]);
      acc[d] = mfma(a, pf, acc[d]);
    }
  }

  u32x2 wo[9][8];
  {
    const unsigned voff = g * (8u * 1152u) + l15 * 8u;
    const unsigned so = kvh * (256u * 1152u);
#pragma unroll
    for (int kb = 0; kb < 8; ++kb)
#pragma unroll
      for (int j = 0; j < 8; ++j) wo[kb][j] = __builtin_amdgcn_raw_buffer_load_b64(srdWO, voff + j * 1152u, so + kb * (32u * 1152u) + wave * 128u, 0);
#pragma unroll
    for (int j = 0; j < 8; ++j) wo[8][j] = __builtin_amdgcn_raw_buffer_load_b64(srdWO, voff + j * 1152u, so + wave * (32u * 1152u) + 1024u, 0);
  }

  lrun = xsum16_32(lrun);
  if (l15 < 8) {
    const unsigned base = kslot + l15 * 512u;
#pragma unroll
    for (int r = 0; r < 4; ++r) {
      lds_st<f32x4>(base + (((8 * g + 2 * r) ^ l15) * 16u), f32x4{acc[0][r], acc[1][r], acc[2][r], acc[3][r]});
      lds_st<f32x4>(base + (((8 * g + 2 * r + 1) ^ l15) * 16u), f32x4{acc[4][r], acc[5][r], acc[6][r], acc[7][r]});
    }
    if (g == 0) lds_st<f32x2>(kslot + 4096u + l15 * 8u, f32x2{mrun, lrun});
  }
  barrier();
  {
    const unsigned q = (tid >> 5) & 7, u = tid & 31, w0 = lds0 + (tid >> 8) * 65536u;
    f32x2 ml[4];
#pragma unroll
    for (int w = 0; w < 4; ++w) ml[w] = lds_ld<f32x2>(w0 + w * 16384u + 4096u + q * 8u);
    const float M = fmaxf(fmaxf(ml[0][0], ml[1][0]), fmaxf(ml[2][0], ml[3][0]));
    float L = 0.f; f32x4 o = {0.f, 0.f, 0.f, 0.f};
#pragma unroll
    for (int w = 0; w < 4; ++w) {
      const float e = __builtin_amdgcn_exp2f((ml[w][0] - M) * SC);
      L = fmaf(e, ml[w][1], L);
      o += e * lds_ld<f32x4>(w0 + w * 16384u + q * 512u + ((u ^ q) * 16u));
    }
    const float inv = 1.0f / L;
    const unsigned m = (tid >> 8) * 4 + (q & 3), un = (q >> 2) * 16 + (u >> 1);
    lds_st<u32x2>(lds0 + LDS_QP + m * 512u + ((un ^ m) * 16u) + (u & 1) * 8u, u32x2{cvtpk(o[0] * inv, o[1] * inv), cvtpk(o[2] * inv, o[3] * inv)});
  }
  barrier();

  {
    const unsigned m = lane & 7;
    const unsigned ob = lds0 + LDS_QP + m * 512u;
    f32x4 oa[4], ra[4];
#pragma unroll
    for (int d = 0; d < 4; ++d) { oa[d] = f32x4{0.f, 0.f, 0.f, 0.f}; ra[d] = f32x4{0.f, 0.f, 0.f, 0.f}; }
#pragma unroll
    for (int kb = 0; kb < 9; ++kb) {
      const unsigned kk = kb < 8 ? (unsigned)kb : wave;
      const u32x4 bf = lds_ld<u32x4>(ob + (((4 * kk + g) ^ m) * 16u));
#pragma unroll
      for (int d = 0; d < 4; ++d) {
        u32x4 a;
#pragma unroll
        for (int u = 0; u < 4; ++u) a[u] = (d & 1) ? perm_hi(wo[kb][2 * u + 1][d >> 1], wo[kb][2 * u][d >> 1]) : perm_lo(wo[kb][2 * u + 1][d >> 1], wo[kb][2 * u][d >> 1]);
        if (kb < 8) oa[d] = mfma(a, bf, oa[d]); else ra[d] = mfma(a, bf, ra[d]);
      }
    }
    if (l15 < 8) {
#pragma unroll
      for (int r = 0; r < 4; ++r) {
        lds_st<f32x4>(lds0 + LDS_OUT + l15 * OUT_ROW + (64 * wave + 16 * g + 4 * r) * 4u, f32x4{oa[0][r], oa[1][r], oa[2][r], oa[3][r]});
        lds_st<f32x4>(lds0 + LDS_REM + (wave * 8 + l15) * 272u + (16 * g + 4 * r) * 4u, f32x4{ra[0][r], ra[1][r], ra[2][r], ra[3][r]});
      }
    }
  }
  barrier();
  {
    float* sl = slab + (size_t)(bp * HKV + kvh) * (8 * DM);
#pragma unroll
    for (int i = 0; i < 2; ++i) {
      const unsigned e = tid + i * 512u, m = e >> 7, c = e & 127;
      *(f32x4*)(sl + m * DM + c * 4) = lds_ld<f32x4>(lds0 + LDS_OUT + m * OUT_ROW + c * 16u);
    }
    if (tid < 128) {
      const unsigned m = tid >> 4, c = tid & 15;
      f32x4 s = {0.f, 0.f, 0.f, 0.f};
#pragma unroll
      for (int w = 0; w < 8; ++w) s += lds_ld<f32x4>(lds0 + LDS_REM + (w * 8 + m) * 272u + c * 16u);
      *(f32x4*)(sl + m * DM + 512 + c * 4) = s;
    }
  }
}

__global__ __launch_bounds__(64) void reduce_kernel(const float* __restrict__ slab, __hip_bfloat16* __restrict__ out) {
  const unsigned e = blockIdx.x * 64 + threadIdx.x;
  const unsigned row = e / 144, c = e % 144, bp = row >> 3, m = row & 7;
  const float* p = slab + (size_t)bp * HKV * (8 * DM) + m * DM + c * 4;
  f32x4 v[HKV];
#pragma unroll
  for (int k = 0; k < HKV; ++k) v[k] = *(const f32x4*)(p + (size_t)k * (8 * DM));
  f32x4 s = v[0];
#pragma unroll
  for (int k = 1; k < HKV; ++k) s += v[k];
  u32x2 o = {cvtpk(s[0], s[1]), cvtpk(s[2], s[3])};
  *(u32x2*)((char*)out + ((size_t)row * DM + c * 4) * 2) = o;
}

}

extern "C" void kernel_launch(void* const* d_in, const int* in_sizes, int n_in, void* d_out, int out_size, void* d_ws, size_t ws_size,
                              hipStream_t stream) {
  (void)in_sizes; (void)n_in; (void)out_size; (void)ws_size;
  (void)hipFuncSetAttribute((const void*)attn_fused_kernel, hipFuncAttributeMaxDynamicSharedMemorySize, LDS_BYTES);
  float* slab = (float*)d_ws;
  attn_fused_kernel<<<NWG, NTHR, LDS_BYTES, stream>>>(d_in[0], d_in[1], d_in[2], d_in[3], d_in[4], slab);
  reduce_kernel<<<(NB * SQ * DM / 4) / 64, 64, 0, stream>>>(slab, (__hip_bfloat16*)d_out);
}
```

```cpp
#include <hip/hip_runtime.h>
#include <hip/hip_bf16.h>

#define NPRE 3
#define ROT 1
#define TROT 1
#define WO_N0 8

#define STAMPX(i) do { } while (0)
#define STAMP(i) do { } while (0)
#define STAMP_FLUSH() do { } while (0)

namespace {

constexpr int NB = 32, SQ = 4, DM = 576, HQ = 32, HKV = 16, SK = 2048, DH = 128;
constexpr int NWG = 256, NTHR = 512;
constexpr unsigned KV_BYTES = (unsigned)NB * HKV * SK * DH * 2u;
constexpr unsigned X_BYTES = NB * SQ * DM * 2, WQ_BYTES = DM * HQ * DH * 2, WO_BYTES = HQ * DH * DM * 2;
constexpr int TILES = 32;
constexpr int LDS_QP = 131072;
constexpr int LDS_BYTES = 163840;
constexpr int OUT_ROW = 2320;
constexpr int LDS_OUT = 0, LDS_REM = 8 * OUT_ROW;
constexpr float SC = 0.08838834764831845f * 1.4426950408889634f;
constexpr unsigned WS_EPOCH = 0, WS_FLAG = 4096, WS_SLAB = 65536;
constexpr unsigned SLAB_BYTES = 16u * 16u * 8u * DM * 4u;

typedef unsigned u32x4 __attribute__((ext_vector_type(4)));
typedef unsigned u32x2 __attribute__((ext_vector_type(2)));
typedef short s16x4 __attribute__((ext_vector_type(4)));
typedef float f32x4 __attribute__((ext_vector_type(4)));
typedef float f32x2 __attribute__((ext_vector_type(2)));
typedef __bf16 bf16x8 __attribute__((ext_vector_type(8)));
typedef __bf16 bf16x2 __attribute__((ext_vector_type(2)));
#define LAS __attribute__((address_space(3)))
#define GAS __attribute__((address_space(1)))
#define DI __device__ __forceinline__

DI unsigned rfl(unsigned x) { return __builtin_amdgcn_readfirstlane(x); }
DI unsigned cvtpk(float lo, float hi) { f32x2 v = {lo, hi}; bf16x2 b = __builtin_convertvector(v, bf16x2); return __builtin_bit_cast(unsigned, b); }
DI f32x4 mfma32(u32x4 a, u32x4 b, f32x4 c) { return __builtin_amdgcn_mfma_f32_16x16x32_bf16(__builtin_bit_cast(bf16x8, a), __builtin_bit_cast(bf16x8, b), c, 0, 0, 0); }
DI f32x4 mfma16(u32x2 a, u32x2 b, f32x4 c) { return __builtin_amdgcn_mfma_f32_16x16x16bf16_1k(__builtin_bit_cast(s16x4, a), __builtin_bit_cast(s16x4, b), c, 0, 0, 0); }
DI unsigned perm_lo(unsigned second, unsigned first) { return __builtin_amdgcn_perm(second, first, 0x05040100u); }
DI unsigned perm_hi(unsigned second, unsigned first) { return __builtin_amdgcn_perm(second, first, 0x07060302u); }
DI unsigned perm_sel(int odd, unsigned second, unsigned first) { return odd ? perm_hi(second, first) : perm_lo(second, first); }
template <int N> DI void waitvm() { asm volatile("s_waitcnt vmcnt(%0)" ::"n"(N) : "memory"); }
DI void waitlgkm0() { asm volatile("s_waitcnt lgkmcnt(0)" ::: "memory"); }
DI void barrier() { asm volatile("s_waitcnt lgkmcnt(0)\n\ts_barrier" ::: "memory"); }
DI void fence_sched() { __builtin_amdgcn_sched_barrier(0); }
DI float xmax16_32(float x) {
  auto s = __builtin_amdgcn_permlane16_swap(__float_as_uint(x), __float_as_uint(x), false, false);
  x = fmaxf(__uint_as_float(s[0]), __uint_as_float(s[1]));
  auto t = __builtin_amdgcn_permlane32_swap(__float_as_uint(x), __float_as_uint(x), false, false);
  return fmaxf(__uint_as_float(t[0]), __uint_as_float(t[1]));
}
DI float xsum16_32(float x) {
  auto s = __builtin_amdgcn_permlane16_swap(__float_as_uint(x), __float_as_uint(x), false, false);
  x = __uint_as_float(s[0]) + __uint_as_float(s[1]);
  auto t = __builtin_amdgcn_permlane32_swap(__float_as_uint(x), __float_as_uint(x), false, false);
  return __uint_as_float(t[0]) + __uint_as_float(t[1]);
}
DI void dma4(unsigned m0, unsigned v0, unsigned v1, unsigned v2, unsigned v3, __amdgpu_buffer_rsrc_t srd, unsigned soff) {
  asm volatile("s_waitcnt lgkmcnt(0)\n\ts_nop 1\n\ts_mov_b32 m0, %0\n\ts_nop 0\n\t"
               "buffer_load_dwordx4 %1, %5, %6 offen nt lds\n\t"
               "buffer_load_dwordx4 %2, %5, %6 offen offset:1024 nt lds\n\t"
               "buffer_load_dwordx4 %3, %5, %6 offen offset:2048 nt lds\n\t"
               "buffer_load_dwordx4 %4, %5, %6 offen offset:3072 nt lds"
               ::"s"(m0), "v"(v0), "v"(v1), "v"(v2), "v"(v3), "s"(srd), "s"(soff) : "memory");
}
#define WQ_POL ""
#define WO_POL ""
DI void ld4(u32x4& d, unsigned voff, __amdgpu_buffer_rsrc_t srd, unsigned soff) {
  asm volatile("buffer_load_dwordx4 %0, %1, %2, %3 offen" WQ_POL : "=v"(d) : "v"(voff), "s"(srd), "s"(soff));
}
DI void ld4nt(u32x4& d, unsigned voff, __amdgpu_buffer_rsrc_t srd, unsigned soff) {
  asm volatile("buffer_load_dwordx4 %0, %1, %2, %3 offen nt" : "=v"(d) : "v"(voff), "s"(srd), "s"(soff));
}
DI void ld2(u32x2& d, unsigned voff, __amdgpu_buffer_rsrc_t srd, unsigned soff) {
  asm volatile("buffer_load_dwordx2 %0, %1, %2, %3 offen" WO_POL : "=v"(d) : "v"(voff), "s"(srd), "s"(soff));
}
template <int N> DI void waitvm_pin9(u32x4 (&w)[8], u32x4& x) {
  asm volatile("s_waitcnt vmcnt(%9)" : "+v"(w[0]), "+v"(w[1]), "+v"(w[2]), "+v"(w[3]), "+v"(w[4]), "+v"(w[5]), "+v"(w[6]), "+v"(w[7]), "+v"(x) : "n"(N) : "memory");
}
template <int N> DI void waitvm_pin4(u32x4 (&w)[4]) {
  asm volatile("s_waitcnt vmcnt(%4)" : "+v"(w[0]), "+v"(w[1]), "+v"(w[2]), "+v"(w[3]) : "n"(N) : "memory");
}
template <int N> DI void waitvm_pin8(u32x2 (&w)[8]) {
  asm volatile("s_waitcnt vmcnt(%8)" : "+v"(w[0]), "+v"(w[1]), "+v"(w[2]), "+v"(w[3]), "+v"(w[4]), "+v"(w[5]), "+v"(w[6]), "+v"(w[7]) : "n"(N) : "memory");
}
template <class T> DI T lds_ld(unsigned a) { return *(const LAS T*)(size_t)a; }
template <class T> DI void lds_st(unsigned a, T v) { *(LAS T*)(size_t)a = v; }
DI unsigned ld_agent(const unsigned* p) { return __hip_atomic_load((const GAS unsigned*)p, __ATOMIC_RELAXED, __HIP_MEMORY_SCOPE_AGENT); }
DI void st_agent(unsigned* p, unsigned v) { __hip_atomic_store((GAS unsigned*)p, v, __ATOMIC_RELAXED, __HIP_MEMORY_SCOPE_AGENT); }

__global__ __launch_bounds__(NTHR, 2) void attn_fused_kernel(const void* __restrict__ xg, const void* __restrict__ kg, const void* __restrict__ vg,
                                                            const void* __restrict__ wqg, const void* __restrict__ wog, char* __restrict__ ws,
                                                            void* __restrict__ outg) {
  extern __shared__ __attribute__((aligned(16))) char lds_raw[];
  const unsigned lds0 = (unsigned)(size_t)lds_raw;
  const unsigned tid = threadIdx.x, lane = tid & 63, wave = rfl(tid >> 6);
  const unsigned l15 = lane & 15, g = lane >> 4;
  const unsigned bx = blockIdx.x & 7, bi = blockIdx.x >> 3;
  const unsigned kvh = 2 * bx + (bi & 1), bp = bi >> 1;
  const unsigned bb = wave >> 2, wq4 = wave & 3;
  const unsigned batch = 2 * bp + bb;
  STAMP(0);

  const __amdgpu_buffer_rsrc_t srdK = __builtin_amdgcn_make_buffer_rsrc((void*)kg, (short)0, (int)KV_BYTES, 0x00020000);
  const __amdgpu_buffer_rsrc_t srdV = __builtin_amdgcn_make_buffer_rsrc((void*)vg, (short)0, (int)KV_BYTES, 0x00020000);
  const __amdgpu_buffer_rsrc_t srdX = __builtin_amdgcn_make_buffer_rsrc((void*)xg, (short)0, (int)X_BYTES, 0x00020000);
  const __amdgpu_buffer_rsrc_t srdWQ = __builtin_amdgcn_make_buffer_rsrc((void*)wqg, (short)0, (int)WQ_BYTES, 0x00020000);
  const __amdgpu_buffer_rsrc_t srdWO = __builtin_amdgcn_make_buffer_rsrc((void*)wog, (short)0, (int)WO_BYTES, 0x00020000);

  const unsigned ring = lds0 + wave * 16384u;
  const unsigned tile0 = (batch * HKV + kvh) * (unsigned)(SK * DH * 2) + wq4 * 4096u;
  const unsigned trot = TROT ? bi : 0u;
  auto toff = [&](unsigned n) { return tile0 + ((n + trot) & 31u) * 16384u; };
  unsigned kv[4];
#pragma unroll
  for (int i = 0; i < 4; ++i) kv[i] = g * 256u + ((l15 ^ (4u * i + g)) * 16u);
  const unsigned vv = lane * 16u;
  auto issueK = [&](unsigned n, unsigned par) { dma4(ring + par * 8192u, kv[0], kv[1], kv[2], kv[3], srdK, toff(n)); };
  auto issueV = [&](unsigned n, unsigned par) { dma4(ring + par * 8192u + 4096u, vv, vv, vv, vv, srdV, toff(n)); };
  u32x4 pk[NPRE][4], pv[NPRE][4];
  auto preK = [&](int t) {
#pragma unroll
    for (int i = 0; i < 4; ++i) ld4nt(pk[t][i], kv[i], srdK, toff(2 + t) + i * 1024u);
  };
  auto preV = [&](int t) {
#pragma unroll
    for (int i = 0; i < 4; ++i) ld4nt(pv[t][i], vv, srdV, toff(2 + t) + i * 1024u);
  };

  u32x4 qf[4];
  {
    const unsigned nh = wave & 1, ig = wave >> 1;
    const unsigned ib0 = ig < 2 ? 5 * ig : 10 + 4 * (ig - 2);
    const bool five = ig < 2;
    const unsigned xoff = (8 * bp + (lane & 7)) * (DM * 2) + g * 16u;
    const unsigned woff = g * (8u * 8192u) + l15 * 16u;
    const unsigned rot = ROT ? (bp * 9u) >> 3 : 0u;
    u32x4 xa, xb, wa[8], wb[8];
    const __amdgpu_buffer_rsrc_t srdX5 = __builtin_amdgcn_make_buffer_rsrc((void*)xg, (short)0, five ? (int)X_BYTES : 0, 0x00020000);
    const __amdgpu_buffer_rsrc_t srdWQ5 = __builtin_amdgcn_make_buffer_rsrc((void*)wqg, (short)0, five ? (int)WQ_BYTES : 0, 0x00020000);
    auto loadblk = [&](int k, u32x4 (&w)[8], u32x4& x) {
      unsigned ib = ib0 + k + rot;
      ib = ib >= 18u ? ib - 18u : ib;
      ld4(x, xoff, k == 4 ? srdX5 : srdX, ib * 64u);
      const unsigned wso = ib * (32u * 8192u) + kvh * 512u + nh * 256u;
#pragma unroll
      for (int j = 0; j < 8; ++j) ld4(w[j], woff, k == 4 ? srdWQ5 : srdWQ, wso + j * 8192u);
    };
    f32x4 qa[8];
#pragma unroll
    for (int d = 0; d < 8; ++d) qa[d] = f32x4{0.f, 0.f, 0.f, 0.f};
    auto mmblk = [&](u32x4 (&w)[8], u32x4& x) {
#pragma unroll
      for (int d = 0; d < 8; ++d) {
        u32x4 a;
#pragma unroll
        for (int u = 0; u < 4; ++u) a[u] = perm_sel(d & 1, w[2 * u + 1][d >> 1], w[2 * u][d >> 1]);
        qa[d] = mfma32(a, x, qa[d]);
      }
#pragma unroll
      for (int d = 0; d < 8; ++d) asm volatile("" : "+v"(qa[d]));
      fence_sched();
    };
    issueK(0, 0); loadblk(0, wa, xa); issueV(0, 0); loadblk(1, wb, xb);
    waitvm_pin9<13>(wa, xa); mmblk(wa, xa);
    loadblk(2, wa, xa); issueK(1, 1);
    waitvm_pin9<13>(wb, xb); mmblk(wb, xb);
    loadblk(3, wb, xb); issueV(1, 1);
    waitvm_pin9<17>(wa, xa); mmblk(wa, xa);
    loadblk(4, wa, xa); preK(0); preV(0);
    waitvm_pin9<21>(wb, xb); mmblk(wb, xb);
    preK(1); preV(1);
    waitvm_pin9<16>(wa, xa); mmblk(wa, xa);
    preK(2); preV(2);
    if (l15 < 8) {
      const unsigned base = lds0 + LDS_QP + (ig * 2 + nh) * 4096u + l15 * 512u, key = l15 ^ (nh << 2);
#pragma unroll
      for (int r = 0; r < 4; ++r) {
        lds_st<f32x4>(base + (((8 * g + 2 * r) ^ key) * 16u), f32x4{qa[0][r], qa[1][r], qa[2][r], qa[3][r]});
        lds_st<f32x4>(base + (((8 * g + 2 * r + 1) ^ key) * 16u), f32x4{qa[4][r], qa[5][r], qa[6][r], qa[7][r]});
      }
    }
    STAMP(1);
    barrier();
    STAMP(2);
    const unsigned q = lane & 7, qh = q >> 2, m = bb * 4 + (q & 3), key = m ^ (qh << 2);
    const unsigned rb = lds0 + LDS_QP + qh * 4096u + m * 512u;
#pragma unroll
    for (int st = 0; st < 4; ++st) {
      f32x4 a = {0.f, 0.f, 0.f, 0.f}, b = {0.f, 0.f, 0.f, 0.f};
#pragma unroll
      for (int i = 0; i < 4; ++i) {
        a += lds_ld<f32x4>(rb + i * 8192u + (((8 * st + 2 * g) ^ key) * 16u));
        b += lds_ld<f32x4>(rb + i * 8192u + (((8 * st + 2 * g + 1) ^ key) * 16u));
      }
      qf[st] = u32x4{cvtpk(a[0], a[1]), cvtpk(a[2], a[3]), cvtpk(b[0], b[1]), cvtpk(b[2], b[3])};
    }
  }

  f32x4 acc[8];
#pragma unroll
  for (int d = 0; d < 8; ++d) acc[d] = f32x4{0.f, 0.f, 0.f, 0.f};
  float mrun = -INFINITY, lrun = 0.f;
  const unsigned kra = ring + l15 * 256u + ((g ^ l15) * 16u);
  const unsigned vra = ring + 4096u + g * 1024u + l15 * 16u;

#define TILE_BODY(PAR, WAITK, AFTERK, WAITV, AFTERV)                                                                      \
  {                                                                                                                        \
    WAITK;                                                                                                                 \
    u32x4 kf[4];                                                                                                           \
    _Pragma("unroll") for (int st = 0; st < 4; ++st) kf[st] = lds_ld<u32x4>((kra ^ (st << 6)) + (PAR) * 8192u);           \
    AFTERK;                                                                                                                \
    f32x4 s0 = {0.f, 0.f, 0.f, 0.f};                                                                                       \
    _Pragma("unroll") for (int st = 0; st < 4; ++st) s0 = mfma32(kf[st], qf[st], s0);                                      \
    float mx = xmax16_32(fmaxf(fmaxf(s0[0], s0[1]), fmaxf(s0[2], s0[3])));                                                 \
    const float mnew = fmaxf(mrun, mx);                                                                                    \
    const float alpha = __builtin_amdgcn_exp2f((mrun - mnew) * SC);                                                        \
    const float nm = -mnew * SC;                                                                                           \
    mrun = mnew;                                                                                                           \
    float p[4];                                                                                                            \
    _Pragma("unroll") for (int r = 0; r < 4; ++r) p[r] = __builtin_amdgcn_exp2f(fmaf(s0[r], SC, nm));                      \
    lrun = fmaf(lrun, alpha, (p[0] + p[1]) + (p[2] + p[3]));                                                               \
    const u32x2 pf = {cvtpk(p[0], p[1]), cvtpk(p[2], p[3])};                                                               \
    _Pragma("unroll") for (int d = 0; d < 8; ++d) acc[d] *= alpha;                                                         \
    WAITV;                                                                                                                 \
    u32x4 vr[4];                                                                                                           \
    _Pragma("unroll") for (int j = 0; j < 4; ++j) vr[j] = lds_ld<u32x4>(vra + j * 256u + (PAR) * 8192u);                   \
    AFTERV;                                                                                                                \
    _Pragma("unroll") for (int d = 0; d < 8; ++d) {                                                                        \
      const u32x2 a = {perm_sel(d & 1, vr[1][d >> 1], vr[0][d >> 1]), perm_sel(d & 1, vr[3][d >> 1], vr[2][d >> 1])};      \
      acc[d] = mfma16(a, pf, acc[d]);                                                                                      \
    }                                                                                                                      \
  }

  STAMP(3);
#define STAGE(W, N, DST) { waitvm_pin4<N>(W); waitlgkm0(); _Pragma("unroll") for (int i = 0; i < 4; ++i) lds_st<u32x4>((DST) + i * 1024u + vv, W[i]); }
  constexpr int PX = NPRE == 3 ? 8 : 0;
  TILE_BODY(0u, (void)0, STAGE(pk[0], 12 + PX, ring), (void)0, STAGE(pv[0], 8 + PX, ring + 4096u))
  TILE_BODY(1u, (void)0, STAGE(pk[1], 4 + PX, ring + 8192u), (void)0, STAGE(pv[1], 0 + PX, ring + 8192u + 4096u))
  TILE_BODY(0u, (void)0, STAGE(pk[2], 4, ring), (void)0, STAGE(pv[2], 0, ring + 4096u))
  TILE_BODY(1u, (void)0, issueK(5, 1), (void)0, (void)0)
  TILE_BODY(0u, (void)0, (void)0, (void)0, (void)0)
  constexpr int N0 = 5;
  const unsigned epoch_old = ld_agent((const unsigned*)(ws + WS_EPOCH + bp * 128u));
#define STEADY(n, par) TILE_BODY(par, { waitvm<0>(); issueV(n, par); }, (void)0, { waitvm<0>(); issueK((n) + 1, (par) ^ 1u); }, (void)0)
  for (int n = N0; n < WO_N0; ++n) {
    const unsigned par = n & 1;
    STEADY(n, par)
  }
  STAMP(4);
  u32x4 wo[9][4];
  const unsigned worot = ROT ? bp & 7u : 0u;
  {
    const unsigned voff = g * (8u * 1152u) + l15 * 8u;
    const unsigned so = kvh * (256u * 1152u);
#pragma unroll
    for (int u = 0; u < 9; ++u) {
      const int n = WO_N0 + u;
      constexpr unsigned dummy = 0; (void)dummy;
      const unsigned par = n & 1;
      const unsigned kb = u < 8 ? ((unsigned)u + worot) & 7u : wave;
      const unsigned uso = so + kb * (32u * 1152u) + (u < 8 ? wave * 128u : 1024u);
      u32x2 t[8];
      TILE_BODY(par,
                { waitvm<0>(); _Pragma("unroll") for (int j = 0; j < 8; ++j) ld2(t[j], voff, srdWO, uso + j * 1152u); issueV(n, par); },
                (void)0,
                { waitvm_pin8<0>(t); issueK(n + 1, par ^ 1u);
                  _Pragma("unroll") for (int d = 0; d < 4; ++d) {
                    _Pragma("unroll") for (int i = 0; i < 4; ++i) wo[u][d][i] = perm_sel(d & 1, t[2 * i + 1][d >> 1], t[2 * i][d >> 1]);
                    asm volatile("" : "+v"(wo[u][d]));
                  } },
                (void)0)
    }
  }
  STAMP(5);
  for (int n = WO_N0 + 9; n < TILES - 1; ++n) {
    const unsigned par = n & 1;
    STEADY(n, par)
  }
  TILE_BODY(1u, { waitvm<0>(); issueV(TILES - 1, 1u); }, (void)0, waitvm<0>(), (void)0)
  STAMP(7);

  lrun = xsum16_32(lrun);
  if (l15 < 8) {
    const unsigned base = ring + l15 * 512u;
#pragma unroll
    for (int r = 0; r < 4; ++r) {
      lds_st<f32x4>(base + (((8 * g + 2 * r) ^ l15) * 16u), f32x4{acc[0][r], acc[1][r], acc[2][r], acc[3][r]});
      lds_st<f32x4>(base + (((8 * g + 2 * r + 1) ^ l15) * 16u), f32x4{acc[4][r], acc[5][r], acc[6][r], acc[7][r]});
    }
    if (g == 0) lds_st<f32x2>(ring + 4096u + l15 * 8u, f32x2{mrun, lrun});
  }
  STAMPX(8);
  barrier();
  STAMPX(9);
  {
    const unsigned q = (tid >> 5) & 7, u = tid & 31, w0 = lds0 + (tid >> 8) * 65536u;
    f32x2 ml[4];
#pragma unroll
    for (int w = 0; w < 4; ++w) ml[w] = lds_ld<f32x2>(w0 + w * 16384u + 4096u + q * 8u);
    const float M = fmaxf(fmaxf(ml[0][0], ml[1][0]), fmaxf(ml[2][0], ml[3][0]));
    float L = 0.f; f32x4 o = {0.f, 0.f, 0.f, 0.f};
#pragma unroll
    for (int w = 0; w < 4; ++w) {
      const float e = __builtin_amdgcn_exp2f((ml[w][0] - M) * SC);
      L = fmaf(e, ml[w][1], L);
      o += e * lds_ld<f32x4>(w0 + w * 16384u + q * 512u + ((u ^ q) * 16u));
    }
    const float inv = 1.0f / L;
    const unsigned m = (tid >> 8) * 4 + (q & 3), un = (q >> 2) * 16 + (u >> 1);
    lds_st<u32x2>(lds0 + LDS_QP + m * 512u + ((un ^ m) * 16u) + (u & 1) * 8u, u32x2{cvtpk(o[0] * inv, o[1] * inv), cvtpk(o[2] * inv, o[3] * inv)});
  }
  barrier();
  STAMPX(10);

  {
    const unsigned m = lane & 7;
    const unsigned ob = lds0 + LDS_QP + m * 512u;
    f32x4 oa[4], ra[4];
#pragma unroll
    for (int d = 0; d < 4; ++d) { oa[d] = f32x4{0.f, 0.f, 0.f, 0.f}; ra[d] = f32x4{0.f, 0.f, 0.f, 0.f}; }
#pragma unroll
    for (int kb = 0; kb < 9; ++kb) {
      const unsigned kk = kb < 8 ? ((unsigned)kb + worot) & 7u : wave;
      const u32x4 bf = lds_ld<u32x4>(ob + (((4 * kk + g) ^ m) * 16u));
#pragma unroll
      for (int d = 0; d < 4; ++d) {
        if (kb < 8) oa[d] = mfma32(wo[kb][d], bf, oa[d]); else ra[d] = mfma32(wo[kb][d], bf, ra[d]);
      }
    }
    if (l15 < 8) {
#pragma unroll
      for (int r = 0; r < 4; ++r) {
        lds_st<f32x4>(lds0 + LDS_OUT + l15 * OUT_ROW + (64 * wave + 16 * g + 4 * r) * 4u, f32x4{oa[0][r], oa[1][r], oa[2][r], oa[3][r]});
        lds_st<f32x4>(lds0 + LDS_REM + (wave * 8 + l15) * 272u + (16 * g + 4 * r) * 4u, f32x4{ra[0][r], ra[1][r], ra[2][r], ra[3][r]});
      }
    }
  }
  STAMPX(11);
  barrier();
  STAMPX(12);
  const __amdgpu_buffer_rsrc_t srdS = __builtin_amdgcn_make_buffer_rsrc((void*)(ws + WS_SLAB), (short)0, (int)SLAB_BYTES, 0x00020000);
  {
    const unsigned sl = (bp * HKV + kvh) * (8u * DM * 4u);
    constexpr int AUX = 16;
#pragma unroll
    for (int i = 0; i < 2; ++i) {
      const unsigned e = tid + i * 512u, m = e >> 7, c = e & 127;
      __builtin_amdgcn_raw_buffer_store_b128(__builtin_bit_cast(u32x4, lds_ld<f32x4>(lds0 + LDS_OUT + m * OUT_ROW + c * 16u)), srdS, m * (DM * 4u) + c * 16u, sl, AUX);
    }
    if (tid < 128) {
      const unsigned m = tid >> 4, c = tid & 15;
      f32x4 s = {0.f, 0.f, 0.f, 0.f};
#pragma unroll
      for (int w = 0; w < 8; ++w) s += lds_ld<f32x4>(lds0 + LDS_REM + (w * 8 + m) * 272u + c * 16u);
      __builtin_amdgcn_raw_buffer_store_b128(__builtin_bit_cast(u32x4, s), srdS, m * (DM * 4u) + 2048u + c * 16u, sl, AUX);
    }
  }
  STAMP(13);
  waitvm<0>();
  barrier();
  if (wave >= 2) { STAMP_FLUSH(); return; }
  const unsigned epoch = epoch_old + 0x9E3779B9u;
  unsigned* flags = (unsigned*)(ws + WS_FLAG + bp * 128u);
  if (tid == 0) st_agent(flags + kvh, epoch);
  {
    const unsigned long long t0 = __builtin_amdgcn_s_memrealtime();
    bool ok;
    do {
      const unsigned f = ld_agent(flags + l15);
      ok = __all(f == epoch);
      if (!ok) { __builtin_amdgcn_s_sleep(1); if (__builtin_amdgcn_s_memrealtime() - t0 > 2000000ull) break; }
    } while (!ok);
  }
  asm volatile("" ::: "memory");
  STAMP(14);
  if (tid < 72) {
    const unsigned m = kvh >> 1, c0 = (kvh & 1) * 288u + tid * 4u;
    const unsigned off = m * (DM * 4u) + c0 * 4u;
    u32x4 v[HKV];
#pragma unroll
    for (int k = 0; k < HKV; ++k) v[k] = __builtin_amdgcn_raw_buffer_load_b128(srdS, off, (bp * HKV + k) * (8u * DM * 4u), 16);
    f32x4 s = __builtin_bit_cast(f32x4, v[0]);
#pragma unroll
    for (int k = 1; k < HKV; ++k) s += __builtin_bit_cast(f32x4, v[k]);
    *(u32x2*)((char*)outg + ((size_t)(8 * bp + m) * DM + c0) * 2) = u32x2{cvtpk(s[0], s[1]), cvtpk(s[2], s[3])};
  }
  if (tid == 0 && kvh == 0) st_agent((unsigned*)(ws + WS_EPOCH + bp * 128u), epoch);
  STAMP(15);
  STAMP_FLUSH();
}

}

extern "C" void kernel_launch(void* const* d_in, const int* in_sizes, int n_in, void* d_out, int out_size, void* d_ws, size_t ws_size,
                              hipStream_t stream) {
  (void)in_sizes; (void)n_in; (void)out_size; (void)ws_size;
  (void)hipFuncSetAttribute((const void*)attn_fused_kernel, hipFuncAttributeMaxDynamicSharedMemorySize, LDS_BYTES);
  attn_fused_kernel<<<NWG, NTHR, LDS_BYTES, stream>>>(d_in[0], d_in[1], d_in[2], d_in[3], d_in[4], (char*)d_ws, d_out);
}
```

```cpp
#include <hip/hip_runtime.h>
#include <hip/hip_bf16.h>

#define NPRE 4
#define ROT 1
#define TROT 1
#define WO_N0 11
#define LOCKSTEP 1
#define WO_TOUCH 1

#define STAMPQ(i) do { } while (0)
#define STAMPX(i) do { } while (0)
#define STAMP(i) do { } while (0)
#define STAMP_FLUSH() do { } while (0)

namespace {

constexpr int NB = 32, SQ = 4, DM = 576, HQ = 32, HKV = 16, SK = 2048, DH = 128;
constexpr int NWG = 256, NTHR = 512;
constexpr unsigned KV_BYTES = (unsigned)NB * HKV * SK * DH * 2u;
constexpr unsigned X_BYTES = NB * SQ * DM * 2, WQ_BYTES = DM * HQ * DH * 2, WO_BYTES = HQ * DH * DM * 2;
constexpr int TILES = 32;
constexpr int LDS_QP = 131072;
constexpr int LDS_BYTES = 163840;
constexpr int OUT_ROW = 2320;
constexpr int LDS_OUT = 0, LDS_REM = 8 * OUT_ROW;
constexpr float SC = 0.08838834764831845f * 1.4426950408889634f;
constexpr unsigned WS_EPOCH = 0, WS_FLAG = 4096, WS_SLAB = 65536;
constexpr unsigned SLAB_BYTES = 16u * 16u * 8u * DM * 4u;

typedef unsigned u32x4 __attribute__((ext_vector_type(4)));
typedef unsigned u32x2 __attribute__((ext_vector_type(2)));
typedef short s16x4 __attribute__((ext_vector_type(4)));
typedef float f32x4 __attribute__((ext_vector_type(4)));
typedef float f32x2 __attribute__((ext_vector_type(2)));
typedef __bf16 bf16x8 __attribute__((ext_vector_type(8)));
typedef __bf16 bf16x2 __attribute__((ext_vector_type(2)));
#define LAS __attribute__((address_space(3)))
#define GAS __attribute__((address_space(1)))
#define DI __device__ __forceinline__

DI unsigned rfl(unsigned x) { return __builtin_amdgcn_readfirstlane(x); }
DI unsigned cvtpk(float lo, float hi) { f32x2 v = {lo, hi}; bf16x2 b = __builtin_convertvector(v, bf16x2); return __builtin_bit_cast(unsigned, b); }
DI f32x4 mfma32(u32x4 a, u32x4 b, f32x4 c) { return __builtin_amdgcn_mfma_f32_16x16x32_bf16(__builtin_bit_cast(bf16x8, a), __builtin_bit_cast(bf16x8, b), c, 0, 0, 0); }
DI f32x4 mfma16(u32x2 a, u32x2 b, f32x4 c) { return __builtin_amdgcn_mfma_f32_16x16x16bf16_1k(__builtin_bit_cast(s16x4, a), __builtin_bit_cast(s16x4, b), c, 0, 0, 0); }
DI unsigned perm_lo(unsigned second, unsigned first) { return __builtin_amdgcn_perm(second, first, 0x05040100u); }
DI unsigned perm_hi(unsigned second, unsigned first) { return __builtin_amdgcn_perm(second, first, 0x07060302u); }
DI unsigned perm_sel(int odd, unsigned second, unsigned first) { return odd ? perm_hi(second, first) : perm_lo(second, first); }
template <int N> DI void waitvm() { asm volatile("s_waitcnt vmcnt(%0)" ::"n"(N) : "memory"); }
DI void waitlgkm0() { asm volatile("s_waitcnt lgkmcnt(0)" ::: "memory"); }
DI void barrier() { asm volatile("s_waitcnt lgkmcnt(0)\n\ts_barrier" ::: "memory"); }
DI void fence_sched() { __builtin_amdgcn_sched_barrier(0); }
DI float xmax16_32(float x) {
  auto s = __builtin_amdgcn_permlane16_swap(__float_as_uint(x), __float_as_uint(x), false, false);
  x = fmaxf(__uint_as_float(s[0]), __uint_as_float(s[1]));
  auto t = __builtin_amdgcn_permlane32_swap(__float_as_uint(x), __float_as_uint(x), false, false);
  return fmaxf(__uint_as_float(t[0]), __uint_as_float(t[1]));
}
DI float xsum16_32(float x) {
  auto s = __builtin_amdgcn_permlane16_swap(__float_as_uint(x), __float_as_uint(x), false, false);
  x = __uint_as_float(s[0]) + __uint_as_float(s[1]);
  auto t = __builtin_amdgcn_permlane32_swap(__float_as_uint(x), __float_as_uint(x), false, false);
  return __uint_as_float(t[0]) + __uint_as_float(t[1]);
}
DI void dma4(unsigned m0, unsigned v0, unsigned v1, unsigned v2, unsigned v3, __amdgpu_buffer_rsrc_t srd, unsigned soff) {
  asm volatile("s_waitcnt lgkmcnt(0)\n\ts_nop 1\n\ts_mov_b32 m0, %0\n\ts_nop 0\n\t"
               "buffer_load_dwordx4 %1, %5, %6 offen nt lds\n\t"
               "buffer_load_dwordx4 %2, %5, %6 offen offset:1024 nt lds\n\t"
               "buffer_load_dwordx4 %3, %5, %6 offen offset:2048 nt lds\n\t"
               "buffer_load_dwordx4 %4, %5, %6 offen offset:3072 nt lds"
               ::"s"(m0), "v"(v0), "v"(v1), "v"(v2), "v"(v3), "s"(srd), "s"(soff) : "memory");
}
#define WQ_POL ""
#define WO_POL ""
DI void ld4(u32x4& d, unsigned voff, __amdgpu_buffer_rsrc_t srd, unsigned soff) {
  asm volatile("buffer_load_dwordx4 %0, %1, %2, %3 offen" WQ_POL : "=v"(d) : "v"(voff), "s"(srd), "s"(soff));
}
DI void ld4nt(u32x4& d, unsigned voff, __amdgpu_buffer_rsrc_t srd, unsigned soff) {
  asm volatile("buffer_load_dwordx4 %0, %1, %2, %3 offen nt" : "=v"(d) : "v"(voff), "s"(srd), "s"(soff));
}
DI void ld2(u32x2& d, unsigned voff, __amdgpu_buffer_rsrc_t srd, unsigned soff) {
  asm volatile("buffer_load_dwordx2 %0, %1, %2, %3 offen" WO_POL : "=v"(d) : "v"(voff), "s"(srd), "s"(soff));
}
template <int N> DI void waitvm_pin9(u32x4 (&w)[8], u32x4& x) {
  asm volatile("s_waitcnt vmcnt(%9)" : "+v"(w[0]), "+v"(w[1]), "+v"(w[2]), "+v"(w[3]), "+v"(w[4]), "+v"(w[5]), "+v"(w[6]), "+v"(w[7]), "+v"(x) : "n"(N) : "memory");
}
template <int N> DI void waitvm_pin4(u32x4 (&w)[4]) {
  asm volatile("s_waitcnt vmcnt(%4)" : "+v"(w[0]), "+v"(w[1]), "+v"(w[2]), "+v"(w[3]) : "n"(N) : "memory");
}
template <int N> DI void waitvm_pin8(u32x2 (&w)[8]) {
  asm volatile("s_waitcnt vmcnt(%8)" : "+v"(w[0]), "+v"(w[1]), "+v"(w[2]), "+v"(w[3]), "+v"(w[4]), "+v"(w[5]), "+v"(w[6]), "+v"(w[7]) : "n"(N) : "memory");
}
template <class T> DI T lds_ld(unsigned a) { return *(const LAS T*)(size_t)a; }
template <class T> DI void lds_st(unsigned a, T v) { *(LAS T*)(size_t)a = v; }
DI unsigned ld_agent(const unsigned* p) { return __hip_atomic_load((const GAS unsigned*)p, __ATOMIC_RELAXED, __HIP_MEMORY_SCOPE_AGENT); }
DI void st_agent(unsigned* p, unsigned v) { __hip_atomic_store((GAS unsigned*)p, v, __ATOMIC_RELAXED, __HIP_MEMORY_SCOPE_AGENT); }

__global__ __launch_bounds__(NTHR, 2) void attn_fused_kernel(const void* __restrict__ xg, const void* __restrict__ kg, const void* __restrict__ vg,
                                                            const void* __restrict__ wqg, const void* __restrict__ wog, char* __restrict__ ws,
                                                            void* __restrict__ outg) {
  extern __shared__ __attribute__((aligned(16))) char lds_raw[];
  const unsigned lds0 = (unsigned)(size_t)lds_raw;
  const unsigned tid = threadIdx.x, lane = tid & 63, wave = rfl(tid >> 6);
  const unsigned l15 = lane & 15, g = lane >> 4;
  const unsigned bx = blockIdx.x & 7, bi = blockIdx.x >> 3;
  const unsigned kvh = 2 * bx + (bi & 1), bp = bi >> 1;
  const unsigned bb = wave >> 2, wq4 = wave & 3;
  const unsigned batch = 2 * bp + bb;
  STAMP(0);

  const __amdgpu_buffer_rsrc_t srdK = __builtin_amdgcn_make_buffer_rsrc((void*)kg, (short)0, (int)KV_BYTES, 0x00020000);
  const __amdgpu_buffer_rsrc_t srdV = __builtin_amdgcn_make_buffer_rsrc((void*)vg, (short)0, (int)KV_BYTES, 0x00020000);
  const __amdgpu_buffer_rsrc_t srdX = __builtin_amdgcn_make_buffer_rsrc((void*)xg, (short)0, (int)X_BYTES, 0x00020000);
  const __amdgpu_buffer_rsrc_t srdWQ = __builtin_amdgcn_make_buffer_rsrc((void*)wqg, (short)0, (int)WQ_BYTES, 0x00020000);
  const __amdgpu_buffer_rsrc_t srdWO = __builtin_amdgcn_make_buffer_rsrc((void*)wog, (short)0, (int)WO_BYTES, 0x00020000);

  const unsigned ring = lds0 + wave * 16384u;
  const unsigned tile0 = (batch * HKV + kvh) * (unsigned)(SK * DH * 2) + wq4 * 4096u;
  const unsigned trot = TROT ? bi : 0u;
  auto toff = [&](unsigned n) { return tile0 + ((n + trot) & 31u) * 16384u; };
  unsigned kv[4];
#pragma unroll
  for (int i = 0; i < 4; ++i) kv[i] = g * 256u + ((l15 ^ (4u * i + g)) * 16u);
  const unsigned vv = lane * 16u;
  auto issueK = [&](unsigned n, unsigned par) { dma4(ring + par * 8192u, kv[0], kv[1], kv[2], kv[3], srdK, toff(n)); };
  auto issueV = [&](unsigned n, unsigned par) { dma4(ring + par * 8192u + 4096u, vv, vv, vv, vv, srdV, toff(n)); };
  u32x4 pk[NPRE][4], pv[NPRE][4];
  auto preK = [&](int t) {
#pragma unroll
    for (int i = 0; i < 4; ++i) ld4nt(pk[t][i], kv[i], srdK, toff(2 + t) + i * 1024u);
  };
  auto preV = [&](int t) {
#pragma unroll
    for (int i = 0; i < 4; ++i) ld4nt(pv[t][i], vv, srdV, toff(2 + t) + i * 1024u);
  };

  u32x4 qf[4];
  {
    const unsigned nh = wave & 1, ig = wave >> 1;
    const unsigned ib0 = ig < 2 ? 5 * ig : 10 + 4 * (ig - 2);
    const bool five = ig < 2;
    const unsigned xoff = (8 * bp + (lane & 7)) * (DM * 2) + g * 16u;
    const unsigned woff = g * (8u * 8192u) + l15 * 16u;
    const unsigned rot = ROT ? (bp * 9u) >> 3 : 0u;
    u32x4 xa, xb, wa[8], wb[8];
    const __amdgpu_buffer_rsrc_t srdX5 = __builtin_amdgcn_make_buffer_rsrc((void*)xg, (short)0, five ? (int)X_BYTES : 0, 0x00020000);
    const __amdgpu_buffer_rsrc_t srdWQ5 = __builtin_amdgcn_make_buffer_rsrc((void*)wqg, (short)0, five ? (int)WQ_BYTES : 0, 0x00020000);
    auto loadblk = [&](int k, u32x4 (&w)[8], u32x4& x) {
      unsigned ib = ib0 + k + rot;
      ib = ib >= 18u ? ib - 18u : ib;
      ld4(x, xoff, k == 4 ? srdX5 : srdX, ib * 64u);
      const unsigned wso = ib * (32u * 8192u) + kvh * 512u + nh * 256u;
#pragma unroll
      for (int j = 0; j < 8; ++j) ld4(w[j], woff, k == 4 ? srdWQ5 : srdWQ, wso + j * 8192u);
    };
    f32x4 qa[8];
#pragma unroll
    for (int d = 0; d < 8; ++d) qa[d] = f32x4{0.f, 0.f, 0.f, 0.f};
    auto mmblk = [&](u32x4 (&w)[8], u32x4& x) {
#pragma unroll
      for (int d = 0; d < 8; ++d) {
        u32x4 a;
#pragma unroll
        for (int u = 0; u < 4; ++u) a[u] = perm_sel(d & 1, w[2 * u + 1][d >> 1], w[2 * u][d >> 1]);
        qa[d] = mfma32(a, x, qa[d]);
      }
#pragma unroll
      for (int d = 0; d < 8; ++d) asm volatile("" : "+v"(qa[d]));
      fence_sched();
    };
    u32x4 touch = {0, 0, 0, 0};
    {
      const unsigned so = (kvh * 256u + bp * 16u) * 1152u;
      asm volatile("buffer_load_dwordx4 %0, %1, %2, %3 offen\n\tbuffer_load_dwordx4 %0, %1, %2, %3 offen offset:1024\n\tbuffer_load_dwordx4 %0, %1, %2, %4 offen"
                   : "+v"(touch) : "v"(vv), "s"(srdWO), "s"(so + wave * 2048u), "s"(so + 16384u + (wave & 1) * 1024u));
    }
#define STAGE_SYNC() do { if (LOCKSTEP) asm volatile("s_barrier" ::: "memory"); } while (0)
    static_assert(NPRE == 4 || NPRE == 5, "the stage list below prefetches four or five register tiles");
    issueK(0, 0); loadblk(0, wa, xa); STAGE_SYNC();
    loadblk(1, wb, xb); issueV(0, 0); issueK(1, 1);
    waitvm_pin9<17>(wa, xa); STAMPQ(8);
    asm volatile("" : "+v"(touch));
    mmblk(wa, xa); STAGE_SYNC();
    loadblk(2, wa, xa); issueV(1, 1); preK(0);
    waitvm_pin9<25>(wb, xb); STAMPQ(9); mmblk(wb, xb); STAGE_SYNC();
    loadblk(3, wb, xb); preV(0); preK(1);
    waitvm_pin9<25>(wa, xa); STAMPQ(10); mmblk(wa, xa); STAGE_SYNC();
    loadblk(4, wa, xa); preV(1); preK(2);
    waitvm_pin9<25>(wb, xb); STAMPQ(11); mmblk(wb, xb); STAGE_SYNC();
    preV(2); preK(3);
    waitvm_pin9<16>(wa, xa); STAMPQ(12); mmblk(wa, xa);
    preV(3);
    if (l15 < 8) {
      const unsigned base = (lds0 + LDS_QP + (ig * 2 + nh) * 4096u + l15 * 512u + g * 128u) | ((l15 ^ (nh << 2)) << 4);
#pragma unroll
      for (int r = 0; r < 4; ++r) {
        lds_st<f32x4>(base ^ (32u * r), f32x4{qa[0][r], qa[1][r], qa[2][r], qa[3][r]});
        lds_st<f32x4>(base ^ (32u * r + 16u), f32x4{qa[4][r], qa[5][r], qa[6][r], qa[7][r]});
      }
    }
    STAMP(1);
    barrier();
    STAMP(2);
    {
      const unsigned nh2 = lane >> 5, un = lane & 31, key = wave ^ (nh2 << 2);
      const unsigned row = lds0 + LDS_QP + nh2 * 4096u + wave * 512u;
      const unsigned ra = row + ((un ^ key) << 4);
      const f32x4 p0 = lds_ld<f32x4>(ra), p1 = lds_ld<f32x4>(ra + 8192u), p2 = lds_ld<f32x4>(ra + 16384u), p3 = lds_ld<f32x4>(ra + 24576u);
      const f32x4 sum = (p0 + p1) + (p2 + p3);
      lds_st<u32x2>(row + (((un & ~1u) ^ key) << 4) + (un & 1u) * 8u, u32x2{cvtpk(sum[0], sum[1]), cvtpk(sum[2], sum[3])});
    }
    barrier();
    const unsigned q = lane & 7, qh = q >> 2, m = bb * 4 + (q & 3);
    const unsigned rb = (lds0 + LDS_QP + qh * 4096u + m * 512u) | (((2 * g) ^ (m ^ (qh << 2))) << 4);
#pragma unroll
    for (int st = 0; st < 4; ++st) qf[st] = lds_ld<u32x4>(rb ^ (128u * st));
    barrier();
  }

  f32x4 acc[8];
#pragma unroll
  for (int d = 0; d < 8; ++d) acc[d] = f32x4{0.f, 0.f, 0.f, 0.f};
  float mrun = -INFINITY, lrun = 0.f;
  const unsigned kro = l15 * 256u + ((g ^ l15) * 16u);
  const unsigned vro = g * 1024u + l15 * 16u;

#define TILE_BODY(KSLOT, VSLOT, WAITK, AFTERK, WAITV, AFTERV)                                                              \
  {                                                                                                                        \
    WAITK;                                                                                                                 \
    u32x4 kf[4];                                                                                                           \
    _Pragma("unroll") for (int st = 0; st < 4; ++st) kf[st] = lds_ld<u32x4>((KSLOT) + (kro ^ (st << 6)));                 \
    AFTERK;                                                                                                                \
    f32x4 s0 = {0.f, 0.f, 0.f, 0.f};                                                                                       \
    _Pragma("unroll") for (int st = 0; st < 4; ++st) s0 = mfma32(kf[st], qf[st], s0);                                      \
    float mx = xmax16_32(fmaxf(fmaxf(s0[0], s0[1]), fmaxf(s0[2], s0[3])));                                                 \
    const float mnew = fmaxf(mrun, mx);                                                                                    \
    const float alpha = __builtin_amdgcn_exp2f((mrun - mnew) * SC);                                                        \
    const float nm = -mnew * SC;                                                                                           \
    mrun = mnew;                                                                                                           \
    float p[4];                                                                                                            \
    _Pragma("unroll") for (int r = 0; r < 4; ++r) p[r] = __builtin_amdgcn_exp2f(fmaf(s0[r], SC, nm));                      \
    lrun = fmaf(lrun, alpha, (p[0] + p[1]) + (p[2] + p[3]));                                                               \
    const u32x2 pf = {cvtpk(p[0], p[1]), cvtpk(p[2], p[3])};                                                               \
    _Pragma("unroll") for (int d = 0; d < 8; ++d) acc[d] *= alpha;                                                         \
    WAITV;                                                                                                                 \
    u32x4 vr[4];                                                                                                           \
    _Pragma("unroll") for (int j = 0; j < 4; ++j) vr[j] = lds_ld<u32x4>((VSLOT) + vro + j * 256u);                         \
    AFTERV;                                                                                                                \
    _Pragma("unroll") for (int d = 0; d < 8; ++d) {                                                                        \
      const u32x2 a = {perm_sel(d & 1, vr[1][d >> 1], vr[0][d >> 1]), perm_sel(d & 1, vr[3][d >> 1], vr[2][d >> 1])};      \
      acc[d] = mfma16(a, pf, acc[d]);                                                                                      \
    }                                                                                                                      \
  }
#define PAIR_BODY(PAR, WAITK, AFTERK, WAITV, AFTERV) TILE_BODY(ring + (PAR) * 8192u, ring + (PAR) * 8192u + 4096u, WAITK, AFTERK, WAITV, AFTERV)

  STAMP(3);
  constexpr int F = NPRE + 2;
  constexpr unsigned SH = F & 1;
  const unsigned stage = lds0 + LDS_QP + wave * 4096u;
#define STAGE(W, N) { waitvm_pin4<N>(W); waitlgkm0(); _Pragma("unroll") for (int i = 0; i < 4; ++i) lds_st<u32x4>(stage + i * 1024u + vv, W[i]); }
  PAIR_BODY(0u, (void)0, issueK(F, 0), (void)0, issueV(F, 0))
  PAIR_BODY(1u, (void)0, issueK(F + 1, 1), (void)0, issueV(F + 1, 1))
#define STAGED_TILE(t) TILE_BODY(stage, stage, STAGE(pk[t], 4 * (2 * NPRE - 1 - 2 * (t)) + 16), (void)0, STAGE(pv[t], 4 * (2 * NPRE - 2 - 2 * (t)) + 16), (void)0)
  STAGED_TILE(0)
  STAGED_TILE(1)
  STAGED_TILE(2)
  STAGED_TILE(3)
  PAIR_BODY(0u, waitvm<12>(), (void)0, waitvm<8>(), (void)0)
  PAIR_BODY(1u, waitvm<4>(), (void)0, { waitvm<0>(); issueK(F + 2, 0); }, (void)0)
  constexpr int N0 = F + 2;
#define STEADY(n, par) PAIR_BODY(par, { waitvm<0>(); issueV(n, par); }, (void)0, { waitvm<0>(); issueK((n) + 1, (par) ^ 1u); }, (void)0)
  for (int n = N0; n < WO_N0; ++n) {
    const unsigned par = (n + SH) & 1;
    STEADY(n, par)
  }
  STAMP(4);
  u32x4 wo[9][4];
  const unsigned worot = ROT ? bp & 7u : 0u;
  {
    const unsigned voff = g * (8u * 1152u) + l15 * 8u;
    const unsigned so = kvh * (256u * 1152u);
#pragma unroll
    for (int u = 0; u < 9; ++u) {
      const int n = WO_N0 + u;
      constexpr unsigned dummy = 0; (void)dummy;
      const unsigned par = (n + SH) & 1;
      const unsigned kb = u < 8 ? ((unsigned)u + worot) & 7u : wave;
      const unsigned uso = so + kb * (32u * 1152u) + (u < 8 ? wave * 128u : 1024u);
      u32x2 t[8];
      PAIR_BODY(par,
                { waitvm<0>(); _Pragma("unroll") for (int j = 0; j < 8; ++j) ld2(t[j], voff, srdWO, uso + j * 1152u); issueV(n, par); },
                (void)0,
                { waitvm_pin8<0>(t); issueK(n + 1, par ^ 1u);
                  _Pragma("unroll") for (int d = 0; d < 4; ++d) {
                    _Pragma("unroll") for (int i = 0; i < 4; ++i) wo[u][d][i] = perm_sel(d & 1, t[2 * i + 1][d >> 1], t[2 * i][d >> 1]);
                    asm volatile("" : "+v"(wo[u][d]));
                  } },
                (void)0)
    }
  }
  STAMP(5);
  for (int n = WO_N0 + 9; n < TILES - 1; ++n) {
    const unsigned par = (n + SH) & 1;
    STEADY(n, par)
  }
  PAIR_BODY((TILES - 1 + SH) & 1u, { waitvm<0>(); issueV(TILES - 1, (TILES - 1 + SH) & 1u); }, (void)0, waitvm<0>(), (void)0)
  STAMP(7);

  unsigned lane2;
  asm volatile("v_mbcnt_lo_u32_b32 %0, -1, 0\n\tv_mbcnt_hi_u32_b32 %0, -1, %0" : "=v"(lane2));
  const unsigned l15t = lane2 & 15, gt = lane2 >> 4, tidt = wave * 64u + lane2;
  const unsigned epoch_old = ld_agent((const unsigned*)(ws + WS_EPOCH + bp * 128u));
  lrun = xsum16_32(lrun);
  if (l15t < 8) {
    const unsigned base = ring + l15t * 512u;
#pragma unroll
    for (int r = 0; r < 4; ++r) {
      lds_st<f32x4>(base + (((8 * gt + 2 * r) ^ l15t) * 16u), f32x4{acc[0][r], acc[1][r], acc[2][r], acc[3][r]});
      lds_st<f32x4>(base + (((8 * gt + 2 * r + 1) ^ l15t) * 16u), f32x4{acc[4][r], acc[5][r], acc[6][r], acc[7][r]});
    }
    if (gt == 0) lds_st<f32x2>(ring + 4096u + l15t * 8u, f32x2{mrun, lrun});
  }
  STAMPX(8);
  barrier();
  STAMPX(9);
  {
    const unsigned q = (tidt >> 5) & 7, u = tidt & 31, w0 = lds0 + (tidt >> 8) * 65536u;
    f32x2 ml[4];
#pragma unroll
    for (int w = 0; w < 4; ++w) ml[w] = lds_ld<f32x2>(w0 + w * 16384u + 4096u + q * 8u);
    const float M = fmaxf(fmaxf(ml[0][0], ml[1][0]), fmaxf(ml[2][0], ml[3][0]));
    float L = 0.f; f32x4 o = {0.f, 0.f, 0.f, 0.f};
#pragma unroll
    for (int w = 0; w < 4; ++w) {
      const float e = __builtin_amdgcn_exp2f((ml[w][0] - M) * SC);
      L = fmaf(e, ml[w][1], L);
      o += e * lds_ld<f32x4>(w0 + w * 16384u + q * 512u + ((u ^ q) * 16u));
    }
    const float inv = 1.0f / L;
    const unsigned m = (tidt >> 8) * 4 + (q & 3), un = (q >> 2) * 16 + (u >> 1);
    lds_st<u32x2>(lds0 + LDS_QP + m * 512u + ((un ^ m) * 16u) + (u & 1) * 8u, u32x2{cvtpk(o[0] * inv, o[1] * inv), cvtpk(o[2] * inv, o[3] * inv)});
  }
  barrier();
  STAMPX(10);

  {
    const unsigned m = lane2 & 7;
    const unsigned ob = lds0 + LDS_QP + m * 512u;
    f32x4 oa[4], ra[4];
#pragma unroll
    for (int d = 0; d < 4; ++d) { oa[d] = f32x4{0.f, 0.f, 0.f, 0.f}; ra[d] = f32x4{0.f, 0.f, 0.f, 0.f}; }
#pragma unroll
    for (int kb = 0; kb < 9; ++kb) {
      const unsigned kk = kb < 8 ? ((unsigned)kb + worot) & 7u : wave;
      const u32x4 bf = lds_ld<u32x4>(ob + (((4 * kk + gt) ^ m) * 16u));
#pragma unroll
      for (int d = 0; d < 4; ++d) {
        if (kb < 8) oa[d] = mfma32(wo[kb][d], bf, oa[d]); else ra[d] = mfma32(wo[kb][d], bf, ra[d]);
      }
    }
    if (l15t < 8) {
#pragma unroll
      for (int r = 0; r < 4; ++r) {
        lds_st<f32x4>(lds0 + LDS_OUT + l15t * OUT_ROW + (64 * wave + 16 * gt + 4 * r) * 4u, f32x4{oa[0][r], oa[1][r], oa[2][r], oa[3][r]});
        lds_st<f32x4>(lds0 + LDS_REM + (wave * 8 + l15t) * 272u + (16 * gt + 4 * r) * 4u, f32x4{ra[0][r], ra[1][r], ra[2][r], ra[3][r]});
      }
    }
  }
  STAMPX(11);
  barrier();
  STAMPX(12);
  const __amdgpu_buffer_rsrc_t srdS = __builtin_amdgcn_make_buffer_rsrc((void*)(ws + WS_SLAB), (short)0, (int)SLAB_BYTES, 0x00020000);
  {
    const unsigned sl = (bp * HKV + kvh) * (8u * DM * 4u);
    constexpr int AUX = 16;
#pragma unroll
    for (int i = 0; i < 2; ++i) {
      const unsigned e = tidt + i * 512u, m = e >> 7, c = e & 127;
      __builtin_amdgcn_raw_buffer_store_b128(__builtin_bit_cast(u32x4, lds_ld<f32x4>(lds0 + LDS_OUT + m * OUT_ROW + c * 16u)), srdS, m * (DM * 4u) + c * 16u, sl, AUX);
    }
    if (tidt < 128) {
      const unsigned m = tidt >> 4, c = tidt & 15;
      f32x4 s = {0.f, 0.f, 0.f, 0.f};
#pragma unroll
      for (int w = 0; w < 8; ++w) s += lds_ld<f32x4>(lds0 + LDS_REM + (w * 8 + m) * 272u + c * 16u);
      __builtin_amdgcn_raw_buffer_store_b128(__builtin_bit_cast(u32x4, s), srdS, m * (DM * 4u) + 2048u + c * 16u, sl, AUX);
    }
  }
  STAMPX(13);
  waitvm<0>();
  barrier();
  if (wave >= 2) { STAMP_FLUSH(); return; }
  const unsigned epoch = epoch_old + 0x9E3779B9u;
  unsigned* flags = (unsigned*)(ws + WS_FLAG + bp * 128u);
  if (tidt == 0) st_agent(flags + kvh, epoch);
  {
    const unsigned long long t0 = __builtin_amdgcn_s_memrealtime();
    bool ok;
    do {
      const unsigned f = ld_agent(flags + l15t);
      ok = __all(f == epoch);
      if (!ok) { __builtin_amdgcn_s_sleep(1); if (__builtin_amdgcn_s_memrealtime() - t0 > 2000000ull) break; }
    } while (!ok);
  }
  asm volatile("" ::: "memory");
  STAMPX(14);
  if (tidt < 72) {
    const unsigned m = kvh >> 1, c0 = (kvh & 1) * 288u + tidt * 4u;
    const unsigned off = m * (DM * 4u) + c0 * 4u;
    u32x4 v[HKV];
#pragma unroll
    for (int k = 0; k < HKV; ++k) v[k] = __builtin_amdgcn_raw_buffer_load_b128(srdS, off, (bp * HKV + k) * (8u * DM * 4u), 16);
    f32x4 s = __builtin_bit_cast(f32x4, v[0]);
#pragma unroll
    for (int k = 1; k < HKV; ++k) s += __builtin_bit_cast(f32x4, v[k]);
    *(u32x2*)((char*)outg + ((size_t)(8 * bp + m) * DM + c0) * 2) = u32x2{cvtpk(s[0], s[1]), cvtpk(s[2], s[3])};
  }
  if (tidt == 0 && kvh == 0) st_agent((unsigned*)(ws + WS_EPOCH + bp * 128u), epoch);
  STAMP(15);
  STAMP_FLUSH();
}

}

extern "C" void kernel_launch(void* const* d_in, const int* in_sizes, int n_in, void* d_out, int out_size, void* d_ws, size_t ws_size,
                              hipStream_t stream) {
  (void)in_sizes; (void)n_in; (void)out_size; (void)ws_size;
  (void)hipFuncSetAttribute((const void*)attn_fused_kernel, hipFuncAttributeMaxDynamicSharedMemorySize, LDS_BYTES);
  attn_fused_kernel<<<NWG, NTHR, LDS_BYTES, stream>>>(d_in[0], d_in[1], d_in[2], d_in[3], d_in[4], (char*)d_ws, d_out);
}
```

```cpp
#include <hip/hip_runtime.h>
#include <hip/hip_bf16.h>

#define NPRE 4
#define ROT 1
#define TROT 1
#define WO_N0 11
#define NDEEP 8
#define LOCKSTEP 1
#define WO_TOUCH 1

#define STAMPQ(i) do { } while (0)
#define STAMPX(i) do { } while (0)
#define STAMP(i) do { } while (0)
#define STAMP_FLUSH() do { } while (0)

namespace {

constexpr int NB = 32, SQ = 4, DM = 576, HQ = 32, HKV = 16, SK = 2048, DH = 128;
constexpr int NWG = 256, NTHR = 512;
constexpr unsigned KV_BYTES = (unsigned)NB * HKV * SK * DH * 2u;
constexpr unsigned X_BYTES = NB * SQ * DM * 2, WQ_BYTES = DM * HQ * DH * 2, WO_BYTES = HQ * DH * DM * 2;
constexpr int TILES = 32;
constexpr int LDS_QP = 131072;
constexpr int LDS_BYTES = 163840;
constexpr int OUT_ROW = 2320;
constexpr int LDS_OUT = 0, LDS_REM = 8 * OUT_ROW;
constexpr float SC = 0.08838834764831845f * 1.4426950408889634f;
constexpr unsigned WS_EPOCH = 0, WS_FLAG = 4096, WS_SLAB = 65536;
constexpr unsigned SLAB_BYTES = 16u * 16u * 8u * DM * 4u;

typedef unsigned u32x4 __attribute__((ext_vector_type(4)));
typedef unsigned u32x2 __attribute__((ext_vector_type(2)));
typedef short s16x4 __attribute__((ext_vector_type(4)));
typedef float f32x4 __attribute__((ext_vector_type(4)));
typedef float f32x2 __attribute__((ext_vector_type(2)));
typedef __bf16 bf16x8 __attribute__((ext_vector_type(8)));
typedef __bf16 bf16x2 __attribute__((ext_vector_type(2)));
#define LAS __attribute__((address_space(3)))
#define GAS __attribute__((address_space(1)))
#define DI __device__ __forceinline__

DI unsigned rfl(unsigned x) { return __builtin_amdgcn_readfirstlane(x); }
DI unsigned cvtpk(float lo, float hi) { f32x2 v = {lo, hi}; bf16x2 b = __builtin_convertvector(v, bf16x2); return __builtin_bit_cast(unsigned, b); }
DI f32x4 mfma32(u32x4 a, u32x4 b, f32x4 c) { return __builtin_amdgcn_mfma_f32_16x16x32_bf16(__builtin_bit_cast(bf16x8, a), __builtin_bit_cast(bf16x8, b), c, 0, 0, 0); }
DI f32x4 mfma16(u32x2 a, u32x2 b, f32x4 c) { return __builtin_amdgcn_mfma_f32_16x16x16bf16_1k(__builtin_bit_cast(s16x4, a), __builtin_bit_cast(s16x4, b), c, 0, 0, 0); }
DI unsigned perm_lo(unsigned second, unsigned first) { return __builtin_amdgcn_perm(second, first, 0x05040100u); }
DI unsigned perm_hi(unsigned second, unsigned first) { return __builtin_amdgcn_perm(second, first, 0x07060302u); }
DI unsigned perm_sel(int odd, unsigned second, unsigned first) { return odd ? perm_hi(second, first) : perm_lo(second, first); }
template <int N> DI void waitvm() { asm volatile("s_waitcnt vmcnt(%0)" ::"n"(N) : "memory"); }
DI void waitlgkm0() { asm volatile("s_waitcnt lgkmcnt(0)" ::: "memory"); }
DI void barrier() { asm volatile("s_waitcnt lgkmcnt(0)\n\ts_barrier" ::: "memory"); }
DI void fence_sched() { __builtin_amdgcn_sched_barrier(0); }
DI float xmax16_32(float x) {
  auto s = __builtin_amdgcn_permlane16_swap(__float_as_uint(x), __float_as_uint(x), false, false);
  x = fmaxf(__uint_as_float(s[0]), __uint_as_float(s[1]));
  auto t = __builtin_amdgcn_permlane32_swap(__float_as_uint(x), __float_as_uint(x), false, false);
  return fmaxf(__uint_as_float(t[0]), __uint_as_float(t[1]));
}
DI float xsum16_32(float x) {
  auto s = __builtin_amdgcn_permlane16_swap(__float_as_uint(x), __float_as_uint(x), false, false);
  x = __uint_as_float(s[0]) + __uint_as_float(s[1]);
  auto t = __builtin_amdgcn_permlane32_swap(__float_as_uint(x), __float_as_uint(x), false, false);
  return __uint_as_float(t[0]) + __uint_as_float(t[1]);
}
DI void dma4(unsigned m0, unsigned v0, unsigned v1, unsigned v2, unsigned v3, __amdgpu_buffer_rsrc_t srd, unsigned soff) {
  asm volatile("s_waitcnt lgkmcnt(0)\n\ts_nop 1\n\ts_mov_b32 m0, %0\n\ts_nop 0\n\t"
               "buffer_load_dwordx4 %1, %5, %6 offen nt lds\n\t"
               "buffer_load_dwordx4 %2, %5, %6 offen offset:1024 nt lds\n\t"
               "buffer_load_dwordx4 %3, %5, %6 offen offset:2048 nt lds\n\t"
               "buffer_load_dwordx4 %4, %5, %6 offen offset:3072 nt lds"
               ::"s"(m0), "v"(v0), "v"(v1), "v"(v2), "v"(v3), "s"(srd), "s"(soff) : "memory");
}
#define WQ_POL ""
#define WO_POL ""
DI void ld4(u32x4& d, unsigned voff, __amdgpu_buffer_rsrc_t srd, unsigned soff) {
  asm volatile("buffer_load_dwordx4 %0, %1, %2, %3 offen" WQ_POL : "=v"(d) : "v"(voff), "s"(srd), "s"(soff));
}
DI void ld4nt(u32x4& d, unsigned voff, __amdgpu_buffer_rsrc_t srd, unsigned soff) {
  asm volatile("buffer_load_dwordx4 %0, %1, %2, %3 offen nt" : "=v"(d) : "v"(voff), "s"(srd), "s"(soff));
}
DI void ld2(u32x2& d, unsigned voff, __amdgpu_buffer_rsrc_t srd, unsigned soff) {
  asm volatile("buffer_load_dwordx2 %0, %1, %2, %3 offen" WO_POL : "=v"(d) : "v"(voff), "s"(srd), "s"(soff));
}
template <int N> DI void waitvm_pin9(u32x4 (&w)[8], u32x4& x) {
  asm volatile("s_waitcnt vmcnt(%9)" : "+v"(w[0]), "+v"(w[1]), "+v"(w[2]), "+v"(w[3]), "+v"(w[4]), "+v"(w[5]), "+v"(w[6]), "+v"(w[7]), "+v"(x) : "n"(N) : "memory");
}
template <int N> DI void waitvm_pin4(u32x4 (&w)[4]) {
  asm volatile("s_waitcnt vmcnt(%4)" : "+v"(w[0]), "+v"(w[1]), "+v"(w[2]), "+v"(w[3]) : "n"(N) : "memory");
}
template <int N> DI void waitvm_pin8(u32x2 (&w)[8]) {
  asm volatile("s_waitcnt vmcnt(%8)" : "+v"(w[0]), "+v"(w[1]), "+v"(w[2]), "+v"(w[3]), "+v"(w[4]), "+v"(w[5]), "+v"(w[6]), "+v"(w[7]) : "n"(N) : "memory");
}
template <class T> DI T lds_ld(unsigned a) { return *(const LAS T*)(size_t)a; }
template <class T> DI void lds_st(unsigned a, T v) { *(LAS T*)(size_t)a = v; }
DI unsigned ld_agent(const unsigned* p) { return __hip_atomic_load((const GAS unsigned*)p, __ATOMIC_RELAXED, __HIP_MEMORY_SCOPE_AGENT); }
DI void st_agent(unsigned* p, unsigned v) { __hip_atomic_store((GAS unsigned*)p, v, __ATOMIC_RELAXED, __HIP_MEMORY_SCOPE_AGENT); }

__global__ __launch_bounds__(NTHR, 2) void attn_fused_kernel(const void* __restrict__ xg, const void* __restrict__ kg, const void* __restrict__ vg,
                                                            const void* __restrict__ wqg, const void* __restrict__ wog, char* __restrict__ ws,
                                                            void* __restrict__ outg) {
  extern __shared__ __attribute__((aligned(16))) char lds_raw[];
  const unsigned lds0 = (unsigned)(size_t)lds_raw;
  const unsigned tid = threadIdx.x, lane = tid & 63, wave = rfl(tid >> 6);
  const unsigned l15 = lane & 15, g = lane >> 4;
  const unsigned bx = blockIdx.x & 7, bi = blockIdx.x >> 3;
  const unsigned kvh = 2 * bx + (bi & 1), bp = bi >> 1;
  const unsigned bb = wave >> 2, wq4 = wave & 3;
  const unsigned batch = 2 * bp + bb;
  STAMP(0);

  const __amdgpu_buffer_rsrc_t srdK = __builtin_amdgcn_make_buffer_rsrc((void*)kg, (short)0, (int)KV_BYTES, 0x00020000);
  const __amdgpu_buffer_rsrc_t srdV = __builtin_amdgcn_make_buffer_rsrc((void*)vg, (short)0, (int)KV_BYTES, 0x00020000);
  const __amdgpu_buffer_rsrc_t srdX = __builtin_amdgcn_make_buffer_rsrc((void*)xg, (short)0, (int)X_BYTES, 0x00020000);
  const __amdgpu_buffer_rsrc_t srdWQ = __builtin_amdgcn_make_buffer_rsrc((void*)wqg, (short)0, (int)WQ_BYTES, 0x00020000);
  const __amdgpu_buffer_rsrc_t srdWO = __builtin_amdgcn_make_buffer_rsrc((void*)wog, (short)0, (int)WO_BYTES, 0x00020000);

  const unsigned ring = lds0 + wave * 16384u;
  const unsigned tile0 = (batch * HKV + kvh) * (unsigned)(SK * DH * 2) + wq4 * 4096u;
  const unsigned trot = TROT ? bi : 0u;
  auto toff = [&](unsigned n) { return tile0 + ((n + trot) & 31u) * 16384u; };
  unsigned kv[4];
#pragma unroll
  for (int i = 0; i < 4; ++i) kv[i] = g * 256u + ((l15 ^ (4u * i + g)) * 16u);
  const unsigned vv = lane * 16u;
  auto issueK = [&](unsigned n, unsigned par) { dma4(ring + par * 8192u, kv[0], kv[1], kv[2], kv[3], srdK, toff(n)); };
  auto issueV = [&](unsigned n, unsigned par) { dma4(ring + par * 8192u + 4096u, vv, vv, vv, vv, srdV, toff(n)); };
  u32x4 pk[NPRE][4], pv[NPRE][4];
  auto preK = [&](int t) {
#pragma unroll
    for (int i = 0; i < 4; ++i) ld4nt(pk[t][i], kv[i], srdK, toff(2 + t) + i * 1024u);
  };
  auto preV = [&](int t) {
#pragma unroll
    for (int i = 0; i < 4; ++i) ld4nt(pv[t][i], vv, srdV, toff(2 + t) + i * 1024u);
  };

  u32x4 qf[4];
  {
    const unsigned nh = wave & 1, ig = wave >> 1;
    const unsigned ib0 = ig < 2 ? 5 * ig : 10 + 4 * (ig - 2);
    const bool five = ig < 2;
    const unsigned xoff = (8 * bp + (lane & 7)) * (DM * 2) + g * 16u;
    const unsigned woff = g * (8u * 8192u) + l15 * 16u;
    const unsigned rot = ROT ? (bp * 9u) >> 3 : 0u;
    u32x4 xa, xb, wa[8], wb[8];
    const __amdgpu_buffer_rsrc_t srdX5 = __builtin_amdgcn_make_buffer_rsrc((void*)xg, (short)0, five ? (int)X_BYTES : 0, 0x00020000);
    const __amdgpu_buffer_rsrc_t srdWQ5 = __builtin_amdgcn_make_buffer_rsrc((void*)wqg, (short)0, five ? (int)WQ_BYTES : 0, 0x00020000);
    auto loadblk = [&](int k, u32x4 (&w)[8], u32x4& x) {
      unsigned ib = ib0 + k + rot;
      ib = ib >= 18u ? ib - 18u : ib;
      ld4(x, xoff, k == 4 ? srdX5 : srdX, ib * 64u);
      const unsigned wso = ib * (32u * 8192u) + kvh * 512u + nh * 256u;
#pragma unroll
      for (int j = 0; j < 8; ++j) ld4(w[j], woff, k == 4 ? srdWQ5 : srdWQ, wso + j * 8192u);
    };
    f32x4 qa[8];
#pragma unroll
    for (int d = 0; d < 8; ++d) qa[d] = f32x4{0.f, 0.f, 0.f, 0.f};
    auto mmblk = [&](u32x4 (&w)[8], u32x4& x) {
#pragma unroll
      for (int d = 0; d < 8; ++d) {
        u32x4 a;
#pragma unroll
        for (int u = 0; u < 4; ++u) a[u] = perm_sel(d & 1, w[2 * u + 1][d >> 1], w[2 * u][d >> 1]);
        qa[d] = mfma32(a, x, qa[d]);
      }
#pragma unroll
      for (int d = 0; d < 8; ++d) asm volatile("" : "+v"(qa[d]));
      fence_sched();
    };
    u32x4 touch = {0, 0, 0, 0};
    {
      const unsigned so = (kvh * 256u + bp * 16u) * 1152u;
      asm volatile("buffer_load_dwordx4 %0, %1, %2, %3 offen\n\tbuffer_load_dwordx4 %0, %1, %2, %3 offen offset:1024\n\tbuffer_load_dwordx4 %0, %1, %2, %4 offen"
                   : "+v"(touch) : "v"(vv), "s"(srdWO), "s"(so + wave * 2048u), "s"(so + 16384u + (wave & 1) * 1024u));
    }
#define STAGE_SYNC() do { if (LOCKSTEP) asm volatile("s_barrier" ::: "memory"); } while (0)
    static_assert(NPRE == 4 || NPRE == 5, "the stage list below prefetches four or five register tiles");
    issueK(0, 0); loadblk(0, wa, xa); STAGE_SYNC();
    loadblk(1, wb, xb); issueV(0, 0); issueK(1, 1);
    waitvm_pin9<17>(wa, xa); STAMPQ(8);
    asm volatile("" : "+v"(touch));
    mmblk(wa, xa); STAGE_SYNC();
    loadblk(2, wa, xa); issueV(1, 1); preK(0);
    waitvm_pin9<25>(wb, xb); STAMPQ(9); mmblk(wb, xb); STAGE_SYNC();
    loadblk(3, wb, xb); preV(0); preK(1);
    waitvm_pin9<25>(wa, xa); STAMPQ(10); mmblk(wa, xa); STAGE_SYNC();
    loadblk(4, wa, xa); preV(1); preK(2);
    waitvm_pin9<25>(wb, xb); STAMPQ(11); mmblk(wb, xb); STAGE_SYNC();
    preV(2); preK(3);
    waitvm_pin9<16>(wa, xa); STAMPQ(12); mmblk(wa, xa);
    preV(3);
    if (l15 < 8) {
      const unsigned base = (lds0 + LDS_QP + (ig * 2 + nh) * 4096u + l15 * 512u + g * 128u) | ((l15 ^ (nh << 2)) << 4);
#pragma unroll
      for (int r = 0; r < 4; ++r) {
        lds_st<f32x4>(base ^ (32u * r), f32x4{qa[0][r], qa[1][r], qa[2][r], qa[3][r]});
        lds_st<f32x4>(base ^ (32u * r + 16u), f32x4{qa[4][r], qa[5][r], qa[6][r], qa[7][r]});
      }
    }
    STAMP(1);
    barrier();
    STAMP(2);
    {
      const unsigned nh2 = lane >> 5, un = lane & 31, key = wave ^ (nh2 << 2);
      const unsigned row = lds0 + LDS_QP + nh2 * 4096u + wave * 512u;
      const unsigned ra = row + ((un ^ key) << 4);
      const f32x4 p0 = lds_ld<f32x4>(ra), p1 = lds_ld<f32x4>(ra + 8192u), p2 = lds_ld<f32x4>(ra + 16384u), p3 = lds_ld<f32x4>(ra + 24576u);
      const f32x4 sum = (p0 + p1) + (p2 + p3);
      lds_st<u32x2>(row + (((un & ~1u) ^ key) << 4) + (un & 1u) * 8u, u32x2{cvtpk(sum[0], sum[1]), cvtpk(sum[2], sum[3])});
    }
    barrier();
    const unsigned q = lane & 7, qh = q >> 2, m = bb * 4 + (q & 3);
    const unsigned rb = (lds0 + LDS_QP + qh * 4096u + m * 512u) | (((2 * g) ^ (m ^ (qh << 2))) << 4);
#pragma unroll
    for (int st = 0; st < 4; ++st) qf[st] = lds_ld<u32x4>(rb ^ (128u * st));
    barrier();
  }

  f32x4 acc[8];
#pragma unroll
  for (int d = 0; d < 8; ++d) acc[d] = f32x4{0.f, 0.f, 0.f, 0.f};
  float mrun = -INFINITY, lrun = 0.f;
  const unsigned kro = l15 * 256u + ((g ^ l15) * 16u);
  const unsigned vro = g * 1024u + l15 * 16u;

#define TILE_BODY(KSLOT, VSLOT, WAITK, AFTERK, WAITV, AFTERV)                                                              \
  {                                                                                                                        \
    WAITK;                                                                                                                 \
    u32x4 kf[4];                                                                                                           \
    _Pragma("unroll") for (int st = 0; st < 4; ++st) kf[st] = lds_ld<u32x4>((KSLOT) + (kro ^ (st << 6)));                 \
    AFTERK;                                                                                                                \
    f32x4 s0 = {0.f, 0.f, 0.f, 0.f};                                                                                       \
    _Pragma("unroll") for (int st = 0; st < 4; ++st) s0 = mfma32(kf[st], qf[st], s0);                                      \
    float mx = xmax16_32(fmaxf(fmaxf(s0[0], s0[1]), fmaxf(s0[2], s0[3])));                                                 \
    const float mnew = fmaxf(mrun, mx);                                                                                    \
    const float alpha = __builtin_amdgcn_exp2f((mrun - mnew) * SC);                                                        \
    const float nm = -mnew * SC;                                                                                           \
    mrun = mnew;                                                                                                           \
    float p[4];                                                                                                            \
    _Pragma("unroll") for (int r = 0; r < 4; ++r) p[r] = __builtin_amdgcn_exp2f(fmaf(s0[r], SC, nm));                      \
    lrun = fmaf(lrun, alpha, (p[0] + p[1]) + (p[2] + p[3]));                                                               \
    const u32x2 pf = {cvtpk(p[0], p[1]), cvtpk(p[2], p[3])};                                                               \
    _Pragma("unroll") for (int d = 0; d < 8; ++d) acc[d] *= alpha;                                                         \
    WAITV;                                                                                                                 \
    u32x4 vr[4];                                                                                                           \
    _Pragma("unroll") for (int j = 0; j < 4; ++j) vr[j] = lds_ld<u32x4>((VSLOT) + vro + j * 256u);                         \
    AFTERV;                                                                                                                \
    _Pragma("unroll") for (int d = 0; d < 8; ++d) {                                                                        \
      const u32x2 a = {perm_sel(d & 1, vr[1][d >> 1], vr[0][d >> 1]), perm_sel(d & 1, vr[3][d >> 1], vr[2][d >> 1])};      \
      acc[d] = mfma16(a, pf, acc[d]);                                                                                      \
    }                                                                                                                      \
  }
#define PAIR_BODY(PAR, WAITK, AFTERK, WAITV, AFTERV) TILE_BODY(ring + (PAR) * 8192u, ring + (PAR) * 8192u + 4096u, WAITK, AFTERK, WAITV, AFTERV)

  STAMP(3);
  constexpr int F = NPRE + 2;
  constexpr unsigned SH = F & 1;
  const unsigned stage = lds0 + LDS_QP + wave * 4096u;
#define STAGE(W, N) { waitvm_pin4<N>(W); waitlgkm0(); _Pragma("unroll") for (int i = 0; i < 4; ++i) lds_st<u32x4>(stage + i * 1024u + vv, W[i]); }
  PAIR_BODY(0u, (void)0, issueK(F, 0), (void)0, issueV(F, 0))
  PAIR_BODY(1u, (void)0, issueK(F + 1, 1), (void)0, issueV(F + 1, 1))
#define STAGED_TILE(t) TILE_BODY(stage, stage, STAGE(pk[t], 4 * (2 * NPRE - 1 - 2 * (t)) + 16), (void)0, STAGE(pv[t], 4 * (2 * NPRE - 2 - 2 * (t)) + 16), (void)0)
  STAGED_TILE(0)
  STAGED_TILE(1)
  STAGED_TILE(2)
  STAGED_TILE(3)
  PAIR_BODY(0u, waitvm<12>(), (void)0, waitvm<8>(), (void)0)
  PAIR_BODY(1u, waitvm<4>(), (void)0, { waitvm<0>(); issueK(F + 2, 0); }, (void)0)
  constexpr int N0 = F + 2;
#define STEADY(n, par) PAIR_BODY(par, { waitvm<0>(); issueV(n, par); }, (void)0, { waitvm<0>(); issueK((n) + 1, (par) ^ 1u); }, (void)0)
  for (int n = N0; n < WO_N0; ++n) {
    const unsigned par = (n + SH) & 1;
    STEADY(n, par)
  }
  STAMP(4);
  u32x4 wo[9][4];
  const unsigned worot = ROT ? bp & 7u : 0u;
  {
    const unsigned voff = g * (8u * 1152u) + l15 * 8u;
    const unsigned so = kvh * (256u * 1152u);
#pragma unroll
    for (int u = 0; u < 9; ++u) {
      const int n = WO_N0 + u;
      constexpr unsigned dummy = 0; (void)dummy;
      const unsigned par = (n + SH) & 1;
      const unsigned kb = u < 8 ? ((unsigned)u + worot) & 7u : wave;
      const unsigned uso = so + kb * (32u * 1152u) + (u < 8 ? wave * 128u : 1024u);
      u32x2 t[8];
      PAIR_BODY(par,
                { waitvm<0>(); _Pragma("unroll") for (int j = 0; j < 8; ++j) ld2(t[j], voff, srdWO, uso + j * 1152u); issueV(n, par); },
                (void)0,
                { waitvm_pin8<0>(t); issueK(n + 1, par ^ 1u);
                  _Pragma("unroll") for (int d = 0; d < 4; ++d) {
                    _Pragma("unroll") for (int i = 0; i < 4; ++i) wo[u][d][i] = perm_sel(d & 1, t[2 * i + 1][d >> 1], t[2 * i][d >> 1]);
                    asm volatile("" : "+v"(wo[u][d]));
                  } },
                (void)0)
    }
  }
  STAMP(5);
  for (int n = WO_N0 + 9; n < TILES - NDEEP; ++n) {
    const unsigned par = (n + SH) & 1;
    STEADY(n, par)
  }
  {
    constexpr int n = TILES - NDEEP;
    constexpr unsigned par = (n + SH) & 1;
    PAIR_BODY(par, { waitvm<0>(); issueV(n, par); }, issueK(n + 1, par ^ 1u), waitvm<4>(), issueV(n + 1, par ^ 1u))
  }
  for (int n = TILES - NDEEP + 1; n < TILES - 1; ++n) {
    const unsigned par = (n + SH) & 1;
    PAIR_BODY(par, waitvm<4>(), issueK(n + 1, par ^ 1u), waitvm<4>(), issueV(n + 1, par ^ 1u))
  }
  PAIR_BODY((TILES - 1 + SH) & 1u, waitvm<4>(), (void)0, waitvm<0>(), (void)0)
  STAMP(7);

  unsigned lane2;
  asm volatile("v_mbcnt_lo_u32_b32 %0, -1, 0\n\tv_mbcnt_hi_u32_b32 %0, -1, %0" : "=v"(lane2));
  const unsigned l15t = lane2 & 15, gt = lane2 >> 4, tidt = wave * 64u + lane2;
  const unsigned epoch_old = ld_agent((const unsigned*)(ws + WS_EPOCH + bp * 128u));
  lrun = xsum16_32(lrun);
  if (l15t < 8) {
    const unsigned base = ring + l15t * 512u;
#pragma unroll
    for (int r = 0; r < 4; ++r) {
      lds_st<f32x4>(base + (((8 * gt + 2 * r) ^ l15t) * 16u), f32x4{acc[0][r], acc[1][r], acc[2][r], acc[3][r]});
      lds_st<f32x4>(base + (((8 * gt + 2 * r + 1) ^ l15t) * 16u), f32x4{acc[4][r], acc[5][r], acc[6][r], acc[7][r]});
    }
    if (gt == 0) lds_st<f32x2>(ring + 4096u + l15t * 8u, f32x2{mrun, lrun});
  }
  STAMPX(8);
  barrier();
  STAMPX(9);
  {
    const unsigned q = (tidt >> 5) & 7, u = tidt & 31, w0 = lds0 + (tidt >> 8) * 65536u;
    f32x2 ml[4];
#pragma unroll
    for (int w = 0; w < 4; ++w) ml[w] = lds_ld<f32x2>(w0 + w * 16384u + 4096u + q * 8u);
    const float M = fmaxf(fmaxf(ml[0][0], ml[1][0]), fmaxf(ml[2][0], ml[3][0]));
    float L = 0.f; f32x4 o = {0.f, 0.f, 0.f, 0.f};
#pragma unroll
    for (int w = 0; w < 4; ++w) {
      const float e = __builtin_amdgcn_exp2f((ml[w][0] - M) * SC);
      L = fmaf(e, ml[w][1], L);
      o += e * lds_ld<f32x4>(w0 + w * 16384u + q * 512u + ((u ^ q) * 16u));
    }
    const float inv = 1.0f / L;
    const unsigned m = (tidt >> 8) * 4 + (q & 3), un = (q >> 2) * 16 + (u >> 1);
    lds_st<u32x2>(lds0 + LDS_QP + m * 512u + ((un ^ m) * 16u) + (u & 1) * 8u, u32x2{cvtpk(o[0] * inv, o[1] * inv), cvtpk(o[2] * inv, o[3] * inv)});
  }
  barrier();
  STAMPX(10);

  {
    const unsigned m = lane2 & 7;
    const unsigned ob = lds0 + LDS_QP + m * 512u;
    f32x4 oa[4], ra[4];
#pragma unroll
    for (int d = 0; d < 4; ++d) { oa[d] = f32x4{0.f, 0.f, 0.f, 0.f}; ra[d] = f32x4{0.f, 0.f, 0.f, 0.f}; }
#pragma unroll
    for (int kb = 0; kb < 9; ++kb) {
      const unsigned kk = kb < 8 ? ((unsigned)kb + worot) & 7u : wave;
      const u32x4 bf = lds_ld<u32x4>(ob + (((4 * kk + gt) ^ m) * 16u));
#pragma unroll
      for (int d = 0; d < 4; ++d) {
        if (kb < 8) oa[d] = mfma32(wo[kb][d], bf, oa[d]); else ra[d] = mfma32(wo[kb][d], bf, ra[d]);
      }
    }
    if (l15t < 8) {
#pragma unroll
      for (int r = 0; r < 4; ++r) {
        lds_st<f32x4>(lds0 + LDS_OUT + l15t * OUT_ROW + (64 * wave + 16 * gt + 4 * r) * 4u, f32x4{oa[0][r], oa[1][r], oa[2][r], oa[3][r]});
        lds_st<f32x4>(lds0 + LDS_REM + (wave * 8 + l15t) * 272u + (16 * gt + 4 * r) * 4u, f32x4{ra[0][r], ra[1][r], ra[2][r], ra[3][r]});
      }
    }
  }
  STAMPX(11);
  barrier();
  STAMPX(12);
  const __amdgpu_buffer_rsrc_t srdS = __builtin_amdgcn_make_buffer_rsrc((void*)(ws + WS_SLAB), (short)0, (int)SLAB_BYTES, 0x00020000);
  {
    const unsigned sl = (bp * HKV + kvh) * (8u * DM * 4u);
    constexpr int AUX = 16;
#pragma unroll
    for (int i = 0; i < 2; ++i) {
      const unsigned e = tidt + i * 512u, m = e >> 7, c = e & 127;
      __builtin_amdgcn_raw_buffer_store_b128(__builtin_bit_cast(u32x4, lds_ld<f32x4>(lds0 + LDS_OUT + m * OUT_ROW + c * 16u)), srdS, m * (DM * 4u) + c * 16u, sl, AUX);
    }
    if (tidt < 128) {
      const unsigned m = tidt >> 4, c = tidt & 15;
      f32x4 s = {0.f, 0.f, 0.f, 0.f};
#pragma unroll
      for (int w = 0; w < 8; ++w) s += lds_ld<f32x4>(lds0 + LDS_REM + (w * 8 + m) * 272u + c * 16u);
      __builtin_amdgcn_raw_buffer_store_b128(__builtin_bit_cast(u32x4, s), srdS, m * (DM * 4u) + 2048u + c * 16u, sl, AUX);
    }
  }
  STAMPX(13);
  waitvm<0>();
  barrier();
  if (wave >= 2) { STAMP_FLUSH(); return; }
  const unsigned epoch = epoch_old + 0x9E3779B9u;
  unsigned* flags = (unsigned*)(ws + WS_FLAG + bp * 128u);
  if (tidt == 0) st_agent(flags + kvh, epoch);
  {
    const unsigned long long t0 = __builtin_amdgcn_s_memrealtime();
    bool ok;
    do {
      const unsigned f = ld_agent(flags + l15t);
      ok = __all(f == epoch);
      if (!ok) { __builtin_amdgcn_s_sleep(1); if (__builtin_amdgcn_s_memrealtime() - t0 > 2000000ull) break; }
    } while (!ok);
  }
  asm volatile("" ::: "memory");
  STAMPX(14);
  if (tidt < 72) {
    const unsigned m = kvh >> 1, c0 = (kvh & 1) * 288u + tidt * 4u;
    const unsigned off = m * (DM * 4u) + c0 * 4u;
    u32x4 v[HKV];
#pragma unroll
    for (int k = 0; k < HKV; ++k) v[k] = __builtin_amdgcn_raw_buffer_load_b128(srdS, off, (bp * HKV + k) * (8u * DM * 4u), 16);
    f32x4 s = __builtin_bit_cast(f32x4, v[0]);
#pragma unroll
    for (int k = 1; k < HKV; ++k) s += __builtin_bit_cast(f32x4, v[k]);
    *(u32x2*)((char*)outg + ((size_t)(8 * bp + m) * DM + c0) * 2) = u32x2{cvtpk(s[0], s[1]), cvtpk(s[2], s[3])};
  }
  if (tidt == 0 && kvh == 0) st_agent((unsigned*)(ws + WS_EPOCH + bp * 128u), epoch);
  STAMP(15);
  STAMP_FLUSH();
}

}

extern "C" void kernel_launch(void* const* d_in, const int* in_sizes, int n_in, void* d_out, int out_size, void* d_ws, size_t ws_size,
                              hipStream_t stream) {
  (void)in_sizes; (void)n_in; (void)out_size; (void)ws_size;
  (void)hipFuncSetAttribute((const void*)attn_fused_kernel, hipFuncAttributeMaxDynamicSharedMemorySize, LDS_BYTES);
  attn_fused_kernel<<<NWG, NTHR, LDS_BYTES, stream>>>(d_in[0], d_in[1], d_in[2], d_in[3], d_in[4], (char*)d_ws, d_out);
}
```

```cpp
#include <hip/hip_runtime.h>
#include <hip/hip_bf16.h>

#define NPRE 1
#define ROT 1
#define TROT 1
#define WO_N0 8
#define NDEEP 8
#define LOCKSTEP 1
#define WO_TOUCH 1

#define STAMPQ(i) do { } while (0)
#define STAMPX(i) do { } while (0)
#define STAMP(i) do { } while (0)
#define STAMP_FLUSH() do { } while (0)

namespace {

constexpr int NB = 32, SQ = 4, DM = 576, HQ = 32, HKV = 16, SK = 2048, DH = 128;
constexpr int NWG = 256, NTHR = 512;
constexpr unsigned KV_BYTES = (unsigned)NB * HKV * SK * DH * 2u;
constexpr unsigned X_BYTES = NB * SQ * DM * 2, WQ_BYTES = DM * HQ * DH * 2, WO_BYTES = HQ * DH * DM * 2;
constexpr int TILES = 32;
constexpr int LDS_QP = 131072;
constexpr int LDS_BYTES = 163840;
constexpr int OUT_ROW = 2320;
constexpr int LDS_OUT = 0, LDS_REM = 8 * OUT_ROW;
constexpr float SC = 0.08838834764831845f * 1.4426950408889634f;
constexpr unsigned WS_EPOCH = 0, WS_FLAG = 4096, WS_SLAB = 65536;
constexpr unsigned SLAB_BYTES = 16u * 16u * 8u * DM * 4u;

typedef unsigned u32x4 __attribute__((ext_vector_type(4)));
typedef unsigned u32x2 __attribute__((ext_vector_type(2)));
typedef short s16x4 __attribute__((ext_vector_type(4)));
typedef float f32x4 __attribute__((ext_vector_type(4)));
typedef float f32x2 __attribute__((ext_vector_type(2)));
typedef __bf16 bf16x8 __attribute__((ext_vector_type(8)));
typedef __bf16 bf16x2 __attribute__((ext_vector_type(2)));
#define LAS __attribute__((address_space(3)))
#define GAS __attribute__((address_space(1)))
#define DI __device__ __forceinline__

DI unsigned rfl(unsigned x) { return __builtin_amdgcn_readfirstlane(x); }
DI unsigned cvtpk(float lo, float hi) { f32x2 v = {lo, hi}; bf16x2 b = __builtin_convertvector(v, bf16x2); return __builtin_bit_cast(unsigned, b); }
DI f32x4 mfma32(u32x4 a, u32x4 b, f32x4 c) { return __builtin_amdgcn_mfma_f32_16x16x32_bf16(__builtin_bit_cast(bf16x8, a), __builtin_bit_cast(bf16x8, b), c, 0, 0, 0); }
DI f32x4 mfma16(u32x2 a, u32x2 b, f32x4 c) { return __builtin_amdgcn_mfma_f32_16x16x16bf16_1k(__builtin_bit_cast(s16x4, a), __builtin_bit_cast(s16x4, b), c, 0, 0, 0); }
DI unsigned perm_lo(unsigned second, unsigned first) { return __builtin_amdgcn_perm(second, first, 0x05040100u); }
DI unsigned perm_hi(unsigned second, unsigned first) { return __builtin_amdgcn_perm(second, first, 0x07060302u); }
DI unsigned perm_sel(int odd, unsigned second, unsigned first) { return odd ? perm_hi(second, first) : perm_lo(second, first); }
template <int N> DI void waitvm() { asm volatile("s_waitcnt vmcnt(%0)" ::"n"(N) : "memory"); }
DI void waitlgkm0() { asm volatile("s_waitcnt lgkmcnt(0)" ::: "memory"); }
DI void barrier() { asm volatile("s_waitcnt lgkmcnt(0)\n\ts_barrier" ::: "memory"); }
DI void fence_sched() { __builtin_amdgcn_sched_barrier(0); }
DI float xmax16_32(float x) {
  auto s = __builtin_amdgcn_permlane16_swap(__float_as_uint(x), __float_as_uint(x), false, false);
  x = fmaxf(__uint_as_float(s[0]), __uint_as_float(s[1]));
  auto t = __builtin_amdgcn_permlane32_swap(__float_as_uint(x), __float_as_uint(x), false, false);
  return fmaxf(__uint_as_float(t[0]), __uint_as_float(t[1]));
}
DI float xsum16_32(float x) {
  auto s = __builtin_amdgcn_permlane16_swap(__float_as_uint(x), __float_as_uint(x), false, false);
  x = __uint_as_float(s[0]) + __uint_as_float(s[1]);
  auto t = __builtin_amdgcn_permlane32_swap(__float_as_uint(x), __float_as_uint(x), false, false);
  return __uint_as_float(t[0]) + __uint_as_float(t[1]);
}
DI void dma4(unsigned m0, unsigned v0, unsigned v1, unsigned v2, unsigned v3, __amdgpu_buffer_rsrc_t srd, unsigned soff) {
  asm volatile("s_waitcnt lgkmcnt(0)\n\ts_nop 1\n\ts_mov_b32 m0, %0\n\ts_nop 0\n\t"
               "buffer_load_dwordx4 %1, %5, %6 offen nt lds\n\t"
               "buffer_load_dwordx4 %2, %5, %6 offen offset:1024 nt lds\n\t"
               "buffer_load_dwordx4 %3, %5, %6 offen offset:2048 nt lds\n\t"
               "buffer_load_dwordx4 %4, %5, %6 offen offset:3072 nt lds"
               ::"s"(m0), "v"(v0), "v"(v1), "v"(v2), "v"(v3), "s"(srd), "s"(soff) : "memory");
}
#define WQ_POL ""
#define WO_POL ""
DI void ld4(u32x4& d, unsigned voff, __amdgpu_buffer_rsrc_t srd, unsigned soff) {
  asm volatile("buffer_load_dwordx4 %0, %1, %2, %3 offen" WQ_POL : "=v"(d) : "v"(voff), "s"(srd), "s"(soff));
}
DI void ld4nt(u32x4& d, unsigned voff, __amdgpu_buffer_rsrc_t srd, unsigned soff) {
  asm volatile("buffer_load_dwordx4 %0, %1, %2, %3 offen nt" : "=v"(d) : "v"(voff), "s"(srd), "s"(soff));
}
DI void ld2(u32x2& d, unsigned voff, __amdgpu_buffer_rsrc_t srd, unsigned soff) {
  asm volatile("buffer_load_dwordx2 %0, %1, %2, %3 offen" WO_POL : "=v"(d) : "v"(voff), "s"(srd), "s"(soff));
}
template <int N> DI void waitvm_pin9(u32x4 (&w)[8], u32x4& x) {
  asm volatile("s_waitcnt vmcnt(%9)" : "+v"(w[0]), "+v"(w[1]), "+v"(w[2]), "+v"(w[3]), "+v"(w[4]), "+v"(w[5]), "+v"(w[6]), "+v"(w[7]), "+v"(x) : "n"(N) : "memory");
}
template <int N> DI void waitvm_pin8x4(u32x4 (&w)[8]) {
  asm volatile("s_waitcnt vmcnt(%8)" : "+v"(w[0]), "+v"(w[1]), "+v"(w[2]), "+v"(w[3]), "+v"(w[4]), "+v"(w[5]), "+v"(w[6]), "+v"(w[7]) : "n"(N) : "memory");
}
template <int N> DI void waitvm_pin4(u32x4 (&w)[4]) {
  asm volatile("s_waitcnt vmcnt(%4)" : "+v"(w[0]), "+v"(w[1]), "+v"(w[2]), "+v"(w[3]) : "n"(N) : "memory");
}
template <int N> DI void waitvm_pin8(u32x2 (&w)[8]) {
  asm volatile("s_waitcnt vmcnt(%8)" : "+v"(w[0]), "+v"(w[1]), "+v"(w[2]), "+v"(w[3]), "+v"(w[4]), "+v"(w[5]), "+v"(w[6]), "+v"(w[7]) : "n"(N) : "memory");
}
template <class T> DI T lds_ld(unsigned a) { return *(const LAS T*)(size_t)a; }
template <class T> DI void lds_st(unsigned a, T v) { *(LAS T*)(size_t)a = v; }
DI unsigned ld_agent(const unsigned* p) { return __hip_atomic_load((const GAS unsigned*)p, __ATOMIC_RELAXED, __HIP_MEMORY_SCOPE_AGENT); }
DI void st_agent(unsigned* p, unsigned v) { __hip_atomic_store((GAS unsigned*)p, v, __ATOMIC_RELAXED, __HIP_MEMORY_SCOPE_AGENT); }

__global__ __launch_bounds__(NTHR, 2) void attn_fused_kernel(const void* __restrict__ xg, const void* __restrict__ kg, const void* __restrict__ vg,
                                                            const void* __restrict__ wqg, const void* __restrict__ wog, char* __restrict__ ws,
                                                            void* __restrict__ outg) {
  extern __shared__ __attribute__((aligned(16))) char lds_raw[];
  const unsigned lds0 = (unsigned)(size_t)lds_raw;
  const unsigned tid = threadIdx.x, lane = tid & 63, wave = rfl(tid >> 6);
  const unsigned l15 = lane & 15, g = lane >> 4;
  const unsigned bx = blockIdx.x & 7, bi = blockIdx.x >> 3;
  const unsigned kvh = 2 * bx + (bi & 1), bp = bi >> 1;
  const unsigned bb = wave >> 2, wq4 = wave & 3;
  const unsigned batch = 2 * bp + bb;
  STAMP(0);

  const __amdgpu_buffer_rsrc_t srdK = __builtin_amdgcn_make_buffer_rsrc((void*)kg, (short)0, (int)KV_BYTES, 0x00020000);
  const __amdgpu_buffer_rsrc_t srdV = __builtin_amdgcn_make_buffer_rsrc((void*)vg, (short)0, (int)KV_BYTES, 0x00020000);
  const __amdgpu_buffer_rsrc_t srdX = __builtin_amdgcn_make_buffer_rsrc((void*)xg, (short)0, (int)X_BYTES, 0x00020000);
  const __amdgpu_buffer_rsrc_t srdWQ = __builtin_amdgcn_make_buffer_rsrc((void*)wqg, (short)0, (int)WQ_BYTES, 0x00020000);
  const __amdgpu_buffer_rsrc_t srdWO = __builtin_amdgcn_make_buffer_rsrc((void*)wog, (short)0, (int)WO_BYTES, 0x00020000);

  const unsigned ring = lds0 + wave * 16384u;
  const unsigned tile0 = (batch * HKV + kvh) * (unsigned)(SK * DH * 2) + wq4 * 4096u;
  const unsigned trot = TROT ? bi : 0u;
  auto toff = [&](unsigned n) { return tile0 + ((n + trot) & 31u) * 16384u; };
  unsigned kv[4];
#pragma unroll
  for (int i = 0; i < 4; ++i) kv[i] = g * 256u + ((l15 ^ (4u * i + g)) * 16u);
  const unsigned vv = lane * 16u;
  auto issueK = [&](unsigned n, unsigned par) { dma4(ring + par * 8192u, kv[0], kv[1], kv[2], kv[3], srdK, toff(n)); };
  auto issueV = [&](unsigned n, unsigned par) { dma4(ring + par * 8192u + 4096u, vv, vv, vv, vv, srdV, toff(n)); };
  u32x4 pk[NPRE][4], pv[NPRE][4];
  auto preK = [&](int t) {
#pragma unroll
    for (int i = 0; i < 4; ++i) ld4nt(pk[t][i], kv[i], srdK, toff(2 + t) + i * 1024u);
  };
  auto preV = [&](int t) {
#pragma unroll
    for (int i = 0; i < 4; ++i) ld4nt(pv[t][i], vv, srdV, toff(2 + t) + i * 1024u);
  };

  u32x4 qf[4];
  {
    const unsigned nh = wave & 1, ig = wave >> 1;
    const unsigned ib0 = ig < 2 ? 5 * ig : 10 + 4 * (ig - 2);
    const bool five = ig < 2;
    const unsigned woff = g * (8u * 8192u) + l15 * 16u;
    const unsigned rot = ROT ? (bp * 9u) >> 3 : 0u;
    u32x4 w[5][8], xf[5];
    const __amdgpu_buffer_rsrc_t srdWQ5 = __builtin_amdgcn_make_buffer_rsrc((void*)wqg, (short)0, five ? (int)WQ_BYTES : 0, 0x00020000);
    unsigned ibk[5];
#pragma unroll
    for (int k = 0; k < 5; ++k) { const unsigned ib = ib0 + k + rot; ibk[k] = ib >= 18u ? ib - 18u : ib; }
    auto loadblk = [&](int k) {
      const unsigned wso = ibk[k] * (32u * 8192u) + kvh * 512u + nh * 256u;
#pragma unroll
      for (int j = 0; j < 8; ++j) ld4(w[k][j], woff, k == 4 ? srdWQ5 : srdWQ, wso + j * 8192u);
    };
    f32x4 qa[8];
#pragma unroll
    for (int d = 0; d < 8; ++d) qa[d] = f32x4{0.f, 0.f, 0.f, 0.f};
    auto mmblk = [&](int k) {
#pragma unroll
      for (int d = 0; d < 8; ++d) {
        u32x4 a;
#pragma unroll
        for (int u = 0; u < 4; ++u) a[u] = perm_sel(d & 1, w[k][2 * u + 1][d >> 1], w[k][2 * u][d >> 1]);
        qa[d] = mfma32(a, xf[k], qa[d]);
      }
#pragma unroll
      for (int d = 0; d < 8; ++d) asm volatile("" : "+v"(qa[d]));
      fence_sched();
    };
    {
      const unsigned xs = bp * (8u * DM * 2u);
      asm volatile("s_nop 1\n\ts_mov_b32 m0, %0\n\ts_nop 0\n\tbuffer_load_dwordx4 %2, %3, %4 offen lds\n\t"
                   "s_mov_b32 m0, %1\n\ts_nop 0\n\tbuffer_load_dwordx4 %2, %3, %5 offen lds"
                   ::"s"(lds0 + LDS_QP + wave * 1024u), "s"(lds0 + LDS_QP + 8192u), "v"(vv), "s"(srdX), "s"(xs + wave * 1024u), "s"(xs + 8192u) : "memory");
    }
    u32x4 touch = {0, 0, 0, 0};
    {
      const unsigned so = (kvh * 256u + bp * 16u) * 1152u;
      asm volatile("buffer_load_dwordx4 %0, %1, %2, %3 offen\n\tbuffer_load_dwordx4 %0, %1, %2, %3 offen offset:1024\n\tbuffer_load_dwordx4 %0, %1, %2, %4 offen"
                   : "+v"(touch) : "v"(vv), "s"(srdWO), "s"(so + wave * 2048u), "s"(so + 16384u + (wave & 1) * 1024u));
    }
    constexpr int T_ = 3;
    issueK(0, 0); issueV(0, 0);
    waitvm<T_ + 8>();
    barrier();
    {
      const unsigned xa = lds0 + LDS_QP + (lane & 7) * (DM * 2u) + g * 16u;
#pragma unroll
      for (int k = 0; k < 5; ++k) xf[k] = lds_ld<u32x4>(xa + ibk[k] * 64u);
    }
    barrier();
    loadblk(0); loadblk(1); loadblk(2); loadblk(3); loadblk(4);
    issueK(1, 1); issueV(1, 1);
    preK(0); preV(0);
    waitvm_pin8x4<32 + 8 + 8 * NPRE>(w[0]); STAMPQ(8);
    asm volatile("" : "+v"(touch));
    mmblk(0);
    waitvm_pin8x4<24 + 8 + 8 * NPRE>(w[1]); STAMPQ(9); mmblk(1);
    waitvm_pin8x4<16 + 8 + 8 * NPRE>(w[2]); STAMPQ(10); mmblk(2);
    waitvm_pin8x4<8 + 8 + 8 * NPRE>(w[3]); STAMPQ(11); mmblk(3);
    waitvm_pin8x4<8 + 8 * NPRE>(w[4]); STAMPQ(12); mmblk(4);
    if (l15 < 8) {
      const unsigned base = (lds0 + LDS_QP + (ig * 2 + nh) * 4096u + l15 * 512u + g * 128u) | ((l15 ^ (nh << 2)) << 4);
#pragma unroll
      for (int r = 0; r < 4; ++r) {
        lds_st<f32x4>(base ^ (32u * r), f32x4{qa[0][r], qa[1][r], qa[2][r], qa[3][r]});
        lds_st<f32x4>(base ^ (32u * r + 16u), f32x4{qa[4][r], qa[5][r], qa[6][r], qa[7][r]});
      }
    }
    STAMP(1);
    barrier();
    STAMP(2);
    {
      const unsigned nh2 = lane >> 5, un = lane & 31, key = wave ^ (nh2 << 2);
      const unsigned row = lds0 + LDS_QP + nh2 * 4096u + wave * 512u;
      const unsigned ra = row + ((un ^ key) << 4);
      const f32x4 p0 = lds_ld<f32x4>(ra), p1 = lds_ld<f32x4>(ra + 8192u), p2 = lds_ld<f32x4>(ra + 16384u), p3 = lds_ld<f32x4>(ra + 24576u);
      const f32x4 sum = (p0 + p1) + (p2 + p3);
      lds_st<u32x2>(row + (((un & ~1u) ^ key) << 4) + (un & 1u) * 8u, u32x2{cvtpk(sum[0], sum[1]), cvtpk(sum[2], sum[3])});
    }
    barrier();
    const unsigned q = lane & 7, qh = q >> 2, m = bb * 4 + (q & 3);
    const unsigned rb = (lds0 + LDS_QP + qh * 4096u + m * 512u) | (((2 * g) ^ (m ^ (qh << 2))) << 4);
#pragma unroll
    for (int st = 0; st < 4; ++st) qf[st] = lds_ld<u32x4>(rb ^ (128u * st));
    barrier();
  }

  f32x4 acc[8];
#pragma unroll
  for (int d = 0; d < 8; ++d) acc[d] = f32x4{0.f, 0.f, 0.f, 0.f};
  float mrun = -INFINITY, lrun = 0.f;
  const unsigned kro = l15 * 256u + ((g ^ l15) * 16u);
  const unsigned vro = g * 1024u + l15 * 16u;

#define TILE_BODY(KSLOT, VSLOT, WAITK, AFTERK, WAITV, AFTERV)                                                              \
  {                                                                                                                        \
    WAITK;                                                                                                                 \
    u32x4 kf[4];                                                                                                           \
    _Pragma("unroll") for (int st = 0; st < 4; ++st) kf[st] = lds_ld<u32x4>((KSLOT) + (kro ^ (st << 6)));                 \
    AFTERK;                                                                                                                \
    f32x4 s0 = {0.f, 0.f, 0.f, 0.f};                                                                                       \
    _Pragma("unroll") for (int st = 0; st < 4; ++st) s0 = mfma32(kf[st], qf[st], s0);                                      \
    float mx = xmax16_32(fmaxf(fmaxf(s0[0], s0[1]), fmaxf(s0[2], s0[3])));                                                 \
    const float mnew = fmaxf(mrun, mx);                                                                                    \
    const float alpha = __builtin_amdgcn_exp2f((mrun - mnew) * SC);                                                        \
    const float nm = -mnew * SC;                                                                                           \
    mrun = mnew;                                                                                                           \
    float p[4];                                                                                                            \
    _Pragma("unroll") for (int r = 0; r < 4; ++r) p[r] = __builtin_amdgcn_exp2f(fmaf(s0[r], SC, nm));                      \
    lrun = fmaf(lrun, alpha, (p[0] + p[1]) + (p[2] + p[3]));                                                               \
    const u32x2 pf = {cvtpk(p[0], p[1]), cvtpk(p[2], p[3])};                                                               \
    _Pragma("unroll") for (int d = 0; d < 8; ++d) acc[d] *= alpha;                                                         \
    WAITV;                                                                                                                 \
    u32x4 vr[4];                                                                                                           \
    _Pragma("unroll") for (int j = 0; j < 4; ++j) vr[j] = lds_ld<u32x4>((VSLOT) + vro + j * 256u);                         \
    AFTERV;                                                                                                                \
    _Pragma("unroll") for (int d = 0; d < 8; ++d) {                                                                        \
      const u32x2 a = {perm_sel(d & 1, vr[1][d >> 1], vr[0][d >> 1]), perm_sel(d & 1, vr[3][d >> 1], vr[2][d >> 1])};      \
      acc[d] = mfma16(a, pf, acc[d]);                                                                                      \
    }                                                                                                                      \
  }
#define PAIR_BODY(PAR, WAITK, AFTERK, WAITV, AFTERV) TILE_BODY(ring + (PAR) * 8192u, ring + (PAR) * 8192u + 4096u, WAITK, AFTERK, WAITV, AFTERV)

  STAMP(3);
  constexpr int F = NPRE + 2;
  constexpr unsigned SH = F & 1;
  const unsigned stage = lds0 + LDS_QP + wave * 4096u;
#define STAGE(W, N) { waitvm_pin4<N>(W); waitlgkm0(); _Pragma("unroll") for (int i = 0; i < 4; ++i) lds_st<u32x4>(stage + i * 1024u + vv, W[i]); }
  PAIR_BODY(0u, (void)0, issueK(F, 0), (void)0, issueV(F, 0))
  PAIR_BODY(1u, waitvm<4 + 8 * NPRE + 8>(), issueK(F + 1, 1), waitvm<8 * NPRE + 12>(), issueV(F + 1, 1))
#define STAGED_TILE(t) TILE_BODY(stage, stage, STAGE(pk[t], 4 * (2 * NPRE - 1 - 2 * (t)) + 16), (void)0, STAGE(pv[t], 4 * (2 * NPRE - 2 - 2 * (t)) + 16), (void)0)
  STAGED_TILE(0)
  PAIR_BODY(0u, waitvm<12>(), (void)0, waitvm<8>(), (void)0)
  PAIR_BODY(1u, waitvm<4>(), (void)0, { waitvm<0>(); issueK(F + 2, 0); }, (void)0)
  constexpr int N0 = F + 2;
#define STEADY(n, par) PAIR_BODY(par, { waitvm<0>(); issueV(n, par); }, (void)0, { waitvm<0>(); issueK((n) + 1, (par) ^ 1u); }, (void)0)
  for (int n = N0; n < WO_N0; ++n) {
    const unsigned par = (n + SH) & 1;
    STEADY(n, par)
  }
  STAMP(4);
  u32x4 wo[9][4];
  const unsigned worot = ROT ? bp & 7u : 0u;
  {
    const unsigned voff = g * (8u * 1152u) + l15 * 8u;
    const unsigned so = kvh * (256u * 1152u);
#pragma unroll
    for (int u = 0; u < 9; ++u) {
      const int n = WO_N0 + u;
      constexpr unsigned dummy = 0; (void)dummy;
      const unsigned par = (n + SH) & 1;
      const unsigned kb = u < 8 ? ((unsigned)u + worot) & 7u : wave;
      const unsigned uso = so + kb * (32u * 1152u) + (u < 8 ? wave * 128u : 1024u);
      u32x2 t[8];
      PAIR_BODY(par,
                { waitvm<0>(); _Pragma("unroll") for (int j = 0; j < 8; ++j) ld2(t[j], voff, srdWO, uso + j * 1152u); issueV(n, par); },
                (void)0,
                { waitvm_pin8<0>(t); issueK(n + 1, par ^ 1u);
                  _Pragma("unroll") for (int d = 0; d < 4; ++d) {
                    _Pragma("unroll") for (int i = 0; i < 4; ++i) wo[u][d][i] = perm_sel(d & 1, t[2 * i + 1][d >> 1], t[2 * i][d >> 1]);
                    asm volatile("" : "+v"(wo[u][d]));
                  } },
                (void)0)
    }
  }
  STAMP(5);
  for (int n = WO_N0 + 9; n < TILES - NDEEP; ++n) {
    const unsigned par = (n + SH) & 1;
    STEADY(n, par)
  }
  {
    constexpr int n = TILES - NDEEP;
    constexpr unsigned par = (n + SH) & 1;
    PAIR_BODY(par, { waitvm<0>(); issueV(n, par); }, issueK(n + 1, par ^ 1u), waitvm<4>(), issueV(n + 1, par ^ 1u))
  }
  for (int n = TILES - NDEEP + 1; n < TILES - 1; ++n) {
    const unsigned par = (n + SH) & 1;
    PAIR_BODY(par, waitvm<4>(), issueK(n + 1, par ^ 1u), waitvm<4>(), issueV(n + 1, par ^ 1u))
  }
  PAIR_BODY((TILES - 1 + SH) & 1u, waitvm<4>(), (void)0, waitvm<0>(), (void)0)
  STAMP(7);

  unsigned lane2;
  asm volatile("v_mbcnt_lo_u32_b32 %0, -1, 0\n\tv_mbcnt_hi_u32_b32 %0, -1, %0" : "=v"(lane2));
  const unsigned l15t = lane2 & 15, gt = lane2 >> 4, tidt = wave * 64u + lane2;
  const unsigned epoch_old = ld_agent((const unsigned*)(ws + WS_EPOCH + bp * 128u));
  lrun = xsum16_32(lrun);
  if (l15t < 8) {
    const unsigned base = ring + l15t * 512u;
#pragma unroll
    for (int r = 0; r < 4; ++r) {
      lds_st<f32x4>(base + (((8 * gt + 2 * r) ^ l15t) * 16u), f32x4{acc[0][r], acc[1][r], acc[2][r], acc[3][r]});
      lds_st<f32x4>(base + (((8 * gt + 2 * r + 1) ^ l15t) * 16u), f32x4{acc[4][r], acc[5][r], acc[6][r], acc[7][r]});
    }
    if (gt == 0) lds_st<f32x2>(ring + 4096u + l15t * 8u, f32x2{mrun, lrun});
  }
  STAMPX(8);
  barrier();
  STAMPX(9);
  {
    const unsigned q = (tidt >> 5) & 7, u = tidt & 31, w0 = lds0 + (tidt >> 8) * 65536u;
    f32x2 ml[4];
#pragma unroll
    for (int w = 0; w < 4; ++w) ml[w] = lds_ld<f32x2>(w0 + w * 16384u + 4096u + q * 8u);
    const float M = fmaxf(fmaxf(ml[0][0], ml[1][0]), fmaxf(ml[2][0], ml[3][0]));
    float L = 0.f; f32x4 o = {0.f, 0.f, 0.f, 0.f};
#pragma unroll
    for (int w = 0; w < 4; ++w) {
      const float e = __builtin_amdgcn_exp2f((ml[w][0] - M) * SC);
      L = fmaf(e, ml[w][1], L);
      o += e * lds_ld<f32x4>(w0 + w * 16384u + q * 512u + ((u ^ q) * 16u));
    }
    const float inv = 1.0f / L;
    const unsigned m = (tidt >> 8) * 4 + (q & 3), un = (q >> 2) * 16 + (u >> 1);
    lds_st<u32x2>(lds0 + LDS_QP + m * 512u + ((un ^ m) * 16u) + (u & 1) * 8u, u32x2{cvtpk(o[0] * inv, o[1] * inv), cvtpk(o[2] * inv, o[3] * inv)});
  }
  barrier();
  STAMPX(10);

  {
    const unsigned m = lane2 & 7;
    const unsigned ob = lds0 + LDS_QP + m * 512u;
    f32x4 oa[4], ra[4];
#pragma unroll
    for (int d = 0; d < 4; ++d) { oa[d] = f32x4{0.f, 0.f, 0.f, 0.f}; ra[d] = f32x4{0.f, 0.f, 0.f, 0.f}; }
#pragma unroll
    for (int kb = 0; kb < 9; ++kb) {
      const unsigned kk = kb < 8 ? ((unsigned)kb + worot) & 7u : wave;
      const u32x4 bf = lds_ld<u32x4>(ob + (((4 * kk + gt) ^ m) * 16u));
#pragma unroll
      for (int d = 0; d < 4; ++d) {
        if (kb < 8) oa[d] = mfma32(wo[kb][d], bf, oa[d]); else ra[d] = mfma32(wo[kb][d], bf, ra[d]);
      }
    }
    if (l15t < 8) {
#pragma unroll
      for (int r = 0; r < 4; ++r) {
        lds_st<f32x4>(lds0 + LDS_OUT + l15t * OUT_ROW + (64 * wave + 16 * gt + 4 * r) * 4u, f32x4{oa[0][r], oa[1][r], oa[2][r], oa[3][r]});
        lds_st<f32x4>(lds0 + LDS_REM + (wave * 8 + l15t) * 272u + (16 * gt + 4 * r) * 4u, f32x4{ra[0][r], ra[1][r], ra[2][r], ra[3][r]});
      }
    }
  }
  STAMPX(11);
  barrier();
  STAMPX(12);
  const __amdgpu_buffer_rsrc_t srdS = __builtin_amdgcn_make_buffer_rsrc((void*)(ws + WS_SLAB), (short)0, (int)SLAB_BYTES, 0x00020000);
  {
    const unsigned sl = (bp * HKV + kvh) * (8u * DM * 4u);
    constexpr int AUX = 16;
#pragma unroll
    for (int i = 0; i < 2; ++i) {
      const unsigned e = tidt + i * 512u, m = e >> 7, c = e & 127;
      __builtin_amdgcn_raw_buffer_store_b128(__builtin_bit_cast(u32x4, lds_ld<f32x4>(lds0 + LDS_OUT + m * OUT_ROW + c * 16u)), srdS, m * (DM * 4u) + c * 16u, sl, AUX);
    }
    if (tidt < 128) {
      const unsigned m = tidt >> 4, c = tidt & 15;
      f32x4 s = {0.f, 0.f, 0.f, 0.f};
#pragma unroll
      for (int w = 0; w < 8; ++w) s += lds_ld<f32x4>(lds0 + LDS_REM + (w * 8 + m) * 272u + c * 16u);
      __builtin_amdgcn_raw_buffer_store_b128(__builtin_bit_cast(u32x4, s), srdS, m * (DM * 4u) + 2048u + c * 16u, sl, AUX);
    }
  }
  STAMPX(13);
  waitvm<0>();
  barrier();
  if (wave >= 2) { STAMP_FLUSH(); return; }
  const unsigned epoch = epoch_old + 0x9E3779B9u;
  unsigned* flags = (unsigned*)(ws + WS_FLAG + bp * 128u);
  if (tidt == 0) st_agent(flags + kvh, epoch);
  {
    const unsigned long long t0 = __builtin_amdgcn_s_memrealtime();
    bool ok;
    do {
      const unsigned f = ld_agent(flags + l15t);
      ok = __all(f == epoch);
      if (!ok) { __builtin_amdgcn_s_sleep(1); if (__builtin_amdgcn_s_memrealtime() - t0 > 2000000ull) break; }
    } while (!ok);
  }
  asm volatile("" ::: "memory");
  STAMPX(14);
  if (tidt < 72) {
    const unsigned m = kvh >> 1, c0 = (kvh & 1) * 288u + tidt * 4u;
    const unsigned off = m * (DM * 4u) + c0 * 4u;
    u32x4 v[HKV];
#pragma unroll
    for (int k = 0; k < HKV; ++k) v[k] = __builtin_amdgcn_raw_buffer_load_b128(srdS, off, (bp * HKV + k) * (8u * DM * 4u), 16);
    f32x4 s = __builtin_bit_cast(f32x4, v[0]);
#pragma unroll
    for (int k = 1; k < HKV; ++k) s += __builtin_bit_cast(f32x4, v[k]);
    *(u32x2*)((char*)outg + ((size_t)(8 * bp + m) * DM + c0) * 2) = u32x2{cvtpk(s[0], s[1]), cvtpk(s[2], s[3])};
  }
  if (tidt == 0 && kvh == 0) st_agent((unsigned*)(ws + WS_EPOCH + bp * 128u), epoch);
  STAMP(15);
  STAMP_FLUSH();
}

}

extern "C" void kernel_launch(void* const* d_in, const int* in_sizes, int n_in, void* d_out, int out_size, void* d_ws, size_t ws_size,
                              hipStream_t stream) {
  (void)in_sizes; (void)n_in; (void)out_size; (void)ws_size;
  (void)hipFuncSetAttribute((const void*)attn_fused_kernel, hipFuncAttributeMaxDynamicSharedMemorySize, LDS_BYTES);
  attn_fused_kernel<<<NWG, NTHR, LDS_BYTES, stream>>>(d_in[0], d_in[1], d_in[2], d_in[3], d_in[4], (char*)d_ws, d_out);
}
```

```cpp
#include <hip/hip_runtime.h>
#include <hip/hip_bf16.h>

namespace {

constexpr int NB = 32, SQ = 4, DM = 576, HQ = 32, HKV = 16, SK = 2048, DH = 128;
constexpr int NWG = 256, NTHR = 512;
constexpr unsigned KV_BYTES = (unsigned)NB * HKV * SK * DH * 2u;
constexpr unsigned X_BYTES = NB * SQ * DM * 2, WQ_BYTES = DM * HQ * DH * 2, WO_BYTES = HQ * DH * DM * 2;
constexpr int TILES = 32;
constexpr int NPRE = 2;
constexpr int WO_N0 = 8;
constexpr int NDEEP_A = 10, NDEEP_B = 15;
constexpr int LDS_QP = 131072;
constexpr int LDS_BYTES = 163840;
constexpr int OUT_ROW = 2320;
constexpr int LDS_OUT = 0, LDS_REM = 8 * OUT_ROW;
constexpr float SC = 0.08838834764831845f * 1.4426950408889634f;
constexpr unsigned WS_EPOCH = 0, WS_FLAG = 4096, WS_SLAB = 65536;
constexpr unsigned TAG_STEP = 0x9E3779B9u;
constexpr unsigned long long SPIN_LIMIT = 2000000ull;
constexpr unsigned SLAB_BYTES = 16u * 16u * 8u * DM * 4u;

typedef unsigned u32x4 __attribute__((ext_vector_type(4)));
typedef unsigned u32x2 __attribute__((ext_vector_type(2)));
typedef short s16x4 __attribute__((ext_vector_type(4)));
typedef float f32x4 __attribute__((ext_vector_type(4)));
typedef float f32x2 __attribute__((ext_vector_type(2)));
typedef __bf16 bf16x8 __attribute__((ext_vector_type(8)));
typedef __bf16 bf16x2 __attribute__((ext_vector_type(2)));
#define LAS __attribute__((address_space(3)))
#define GAS __attribute__((address_space(1)))
#define DI __device__ __forceinline__

DI unsigned rfl(unsigned x) { return __builtin_amdgcn_readfirstlane(x); }
DI unsigned cvtpk(float lo, float hi) { f32x2 v = {lo, hi}; bf16x2 b = __builtin_convertvector(v, bf16x2); return __builtin_bit_cast(unsigned, b); }
DI f32x4 mfma32(u32x4 a, u32x4 b, f32x4 c) { return __builtin_amdgcn_mfma_f32_16x16x32_bf16(__builtin_bit_cast(bf16x8, a), __builtin_bit_cast(bf16x8, b), c, 0, 0, 0); }
DI f32x4 mfma16(u32x2 a, u32x2 b, f32x4 c) { return __builtin_amdgcn_mfma_f32_16x16x16bf16_1k(__builtin_bit_cast(s16x4, a), __builtin_bit_cast(s16x4, b), c, 0, 0, 0); }
DI unsigned perm_lo(unsigned second, unsigned first) { return __builtin_amdgcn_perm(second, first, 0x05040100u); }
DI unsigned perm_hi(unsigned second, unsigned first) { return __builtin_amdgcn_perm(second, first, 0x07060302u); }
DI unsigned perm_sel(int odd, unsigned second, unsigned first) { return odd ? perm_hi(second, first) : perm_lo(second, first); }
template <int N> DI void waitvm() { asm volatile("s_waitcnt vmcnt(%0)" ::"n"(N > 63 ? 63 : N) : "memory"); }
DI void waitlgkm0() { asm volatile("s_waitcnt lgkmcnt(0)" ::: "memory"); }
DI void barrier() { asm volatile("s_waitcnt lgkmcnt(0)\n\ts_barrier" ::: "memory"); }
DI void fence_sched() { __builtin_amdgcn_sched_barrier(0); }
DI float xmax16_32(float x) {
  auto s = __builtin_amdgcn_permlane16_swap(__float_as_uint(x), __float_as_uint(x), false, false);
  x = fmaxf(__uint_as_float(s[0]), __uint_as_float(s[1]));
  auto t = __builtin_amdgcn_permlane32_swap(__float_as_uint(x), __float_as_uint(x), false, false);
  return fmaxf(__uint_as_float(t[0]), __uint_as_float(t[1]));
}
DI float xsum16_32(float x) {
  auto s = __builtin_amdgcn_permlane16_swap(__float_as_uint(x), __float_as_uint(x), false, false);
  x = __uint_as_float(s[0]) + __uint_as_float(s[1]);
  auto t = __builtin_amdgcn_permlane32_swap(__float_as_uint(x), __float_as_uint(x), false, false);
  return __uint_as_float(t[0]) + __uint_as_float(t[1]);
}
DI void dma4(unsigned m0, unsigned v0, unsigned v1, unsigned v2, unsigned v3, __amdgpu_buffer_rsrc_t srd, unsigned soff) {
  asm volatile("s_waitcnt lgkmcnt(0)\n\ts_nop 1\n\ts_mov_b32 m0, %0\n\ts_nop 0\n\t"
               "buffer_load_dwordx4 %1, %5, %6 offen nt lds\n\t"
               "buffer_load_dwordx4 %2, %5, %6 offen offset:1024 nt lds\n\t"
               "buffer_load_dwordx4 %3, %5, %6 offen offset:2048 nt lds\n\t"
               "buffer_load_dwordx4 %4, %5, %6 offen offset:3072 nt lds"
               ::"s"(m0), "v"(v0), "v"(v1), "v"(v2), "v"(v3), "s"(srd), "s"(soff) : "memory");
}
DI void ld4(u32x4& d, unsigned voff, __amdgpu_buffer_rsrc_t srd, unsigned soff) {
  asm volatile("buffer_load_dwordx4 %0, %1, %2, %3 offen" : "=v"(d) : "v"(voff), "s"(srd), "s"(soff));
}
DI void ld4nt(u32x4& d, unsigned voff, __amdgpu_buffer_rsrc_t srd, unsigned soff) {
  asm volatile("buffer_load_dwordx4 %0, %1, %2, %3 offen nt" : "=v"(d) : "v"(voff), "s"(srd), "s"(soff));
}
DI void ld2(u32x2& d, unsigned voff, __amdgpu_buffer_rsrc_t srd, unsigned soff) {
  asm volatile("buffer_load_dwordx2 %0, %1, %2, %3 offen" : "=v"(d) : "v"(voff), "s"(srd), "s"(soff));
}
template <int N> DI void waitvm_pin8x4(u32x4 (&w)[8]) {
  asm volatile("s_waitcnt vmcnt(%8)" : "+v"(w[0]), "+v"(w[1]), "+v"(w[2]), "+v"(w[3]), "+v"(w[4]), "+v"(w[5]), "+v"(w[6]), "+v"(w[7]) : "n"(N) : "memory");
}
template <int N> DI void waitvm_pin4(u32x4 (&w)[4]) {
  asm volatile("s_waitcnt vmcnt(%4)" : "+v"(w[0]), "+v"(w[1]), "+v"(w[2]), "+v"(w[3]) : "n"(N) : "memory");
}
template <int N> DI void waitvm_pin8(u32x2 (&w)[8]) {
  asm volatile("s_waitcnt vmcnt(%8)" : "+v"(w[0]), "+v"(w[1]), "+v"(w[2]), "+v"(w[3]), "+v"(w[4]), "+v"(w[5]), "+v"(w[6]), "+v"(w[7]) : "n"(N) : "memory");
}
template <class T> DI T lds_ld(unsigned a) { return *(const LAS T*)(size_t)a; }
template <class T> DI void lds_st(unsigned a, T v) { *(LAS T*)(size_t)a = v; }
DI unsigned ld_agent(const unsigned* p) { return __hip_atomic_load((const GAS unsigned*)p, __ATOMIC_RELAXED, __HIP_MEMORY_SCOPE_AGENT); }
DI void st_agent(unsigned* p, unsigned v) { __hip_atomic_store((GAS unsigned*)p, v, __ATOMIC_RELAXED, __HIP_MEMORY_SCOPE_AGENT); }

__global__ __launch_bounds__(NTHR, 2) void attn_fused_kernel(const void* __restrict__ xg, const void* __restrict__ kg, const void* __restrict__ vg,
                                                            const void* __restrict__ wqg, const void* __restrict__ wog, char* __restrict__ ws,
                                                            void* __restrict__ outg) {
  extern __shared__ __attribute__((aligned(16))) char lds_raw[];
  const unsigned lds0 = (unsigned)(size_t)lds_raw;
  const unsigned tid = threadIdx.x, lane = tid & 63, wave = rfl(tid >> 6);
  const unsigned l15 = lane & 15, g = lane >> 4;
  const unsigned bx = blockIdx.x & 7, bi = blockIdx.x >> 3;
  const unsigned kvh = 2 * bx + (bi & 1), bp = bi >> 1;
  const unsigned bb = wave >> 2, wq4 = wave & 3;
  const unsigned batch = 2 * bp + bb;

  const __amdgpu_buffer_rsrc_t srdK = __builtin_amdgcn_make_buffer_rsrc((void*)kg, (short)0, (int)KV_BYTES, 0x00020000);
  const __amdgpu_buffer_rsrc_t srdV = __builtin_amdgcn_make_buffer_rsrc((void*)vg, (short)0, (int)KV_BYTES, 0x00020000);
  const __amdgpu_buffer_rsrc_t srdX = __builtin_amdgcn_make_buffer_rsrc((void*)xg, (short)0, (int)X_BYTES, 0x00020000);
  const __amdgpu_buffer_rsrc_t srdWQ = __builtin_amdgcn_make_buffer_rsrc((void*)wqg, (short)0, (int)WQ_BYTES, 0x00020000);
  const __amdgpu_buffer_rsrc_t srdWO = __builtin_amdgcn_make_buffer_rsrc((void*)wog, (short)0, (int)WO_BYTES, 0x00020000);

  const unsigned ring = lds0 + wave * 16384u;
  const unsigned tile0 = (batch * HKV + kvh) * (unsigned)(SK * DH * 2) + wq4 * 4096u;
  const unsigned trot = bi;
  auto toff = [&](unsigned n) { return tile0 + ((n + trot) & 31u) * 16384u; };
  unsigned kv[4];
#pragma unroll
  for (int i = 0; i < 4; ++i) kv[i] = g * 256u + ((l15 ^ (4u * i + g)) * 16u);
  const unsigned vv = lane * 16u;
  auto issueK = [&](unsigned n, unsigned par) { dma4(ring + par * 8192u, kv[0], kv[1], kv[2], kv[3], srdK, toff(n)); };
  auto issueV = [&](unsigned n, unsigned par) { dma4(ring + par * 8192u + 4096u, vv, vv, vv, vv, srdV, toff(n)); };
  u32x4 pk[NPRE][4], pv[NPRE][4];
  auto preK = [&](int t) {
#pragma unroll
    for (int i = 0; i < 4; ++i) ld4nt(pk[t][i], kv[i], srdK, toff(2 + t) + i * 1024u);
  };
  auto preV = [&](int t) {
#pragma unroll
    for (int i = 0; i < 4; ++i) ld4nt(pv[t][i], vv, srdV, toff(2 + t) + i * 1024u);
  };

  u32x4 qf[4];
  {
    const unsigned nh = wave & 1, ig = wave >> 1;
    const unsigned ib0 = ig < 2 ? 5 * ig : 10 + 4 * (ig - 2);
    const bool five = ig < 2;
    const unsigned woff = g * (8u * 8192u) + l15 * 16u;
    const unsigned rot = (bp * 9u) >> 3;
    u32x4 w[5][8];
    const unsigned xa = lds0 + LDS_QP + (lane & 7) * (DM * 2u) + g * 16u;
    const __amdgpu_buffer_rsrc_t srdWQ5 = __builtin_amdgcn_make_buffer_rsrc((void*)wqg, (short)0, five ? (int)WQ_BYTES : 0, 0x00020000);
    unsigned ibk[5];
#pragma unroll
    for (int k = 0; k < 5; ++k) { const unsigned ib = ib0 + k + rot; ibk[k] = ib >= 18u ? ib - 18u : ib; }
    auto loadblk = [&](int k) {
      const unsigned wso = ibk[k] * (32u * 8192u) + kvh * 512u + nh * 256u;
#pragma unroll
      for (int j = 0; j < 8; ++j) ld4(w[k][j], woff, k == 4 ? srdWQ5 : srdWQ, wso + j * 8192u);
    };
    f32x4 qa[8];
#pragma unroll
    for (int d = 0; d < 8; ++d) qa[d] = f32x4{0.f, 0.f, 0.f, 0.f};
    auto mmblk = [&](int k) {
      const u32x4 xk = lds_ld<u32x4>(xa + ibk[k] * 64u);
#pragma unroll
      for (int d = 0; d < 8; ++d) {
        u32x4 a;
#pragma unroll
        for (int u = 0; u < 4; ++u) a[u] = perm_sel(d & 1, w[k][2 * u + 1][d >> 1], w[k][2 * u][d >> 1]);
        qa[d] = mfma32(a, xk, qa[d]);
      }
#pragma unroll
      for (int d = 0; d < 8; ++d) asm volatile("" : "+v"(qa[d]));
      fence_sched();
    };
    {
      const unsigned xs = bp * (8u * DM * 2u);
      asm volatile("s_nop 1\n\ts_mov_b32 m0, %0\n\ts_nop 0\n\tbuffer_load_dwordx4 %2, %3, %4 offen lds\n\t"
                   "s_mov_b32 m0, %1\n\ts_nop 0\n\tbuffer_load_dwordx4 %2, %3, %5 offen lds"
                   ::"s"(lds0 + LDS_QP + wave * 1024u), "s"(lds0 + LDS_QP + 8192u), "v"(vv), "s"(srdX), "s"(xs + wave * 1024u), "s"(xs + 8192u) : "memory");
    }
    u32x4 touch = {0, 0, 0, 0};
    {
      const unsigned so = (kvh * 256u + bp * 16u) * 1152u;
      asm volatile("buffer_load_dwordx4 %0, %1, %2, %3 offen\n\tbuffer_load_dwordx4 %0, %1, %2, %3 offen offset:1024\n\tbuffer_load_dwordx4 %0, %1, %2, %4 offen"
                   : "+v"(touch) : "v"(vv), "s"(srdWO), "s"(so + wave * 2048u), "s"(so + 16384u + (wave & 1) * 1024u));
    }
    constexpr int T_ = 3;
    if ((bp & 1) == 0) {
      loadblk(0); loadblk(1); loadblk(2); loadblk(3); loadblk(4);
      asm volatile("s_barrier" ::: "memory");
      issueK(0, 0); issueV(0, 0);
      waitvm<T_ + 40 + 8>();
      barrier();
      waitvm_pin8x4<32 + 8>(w[0]); mmblk(0);
      waitvm_pin8x4<24 + 8>(w[1]); mmblk(1); issueK(1, 1);
      waitvm_pin8x4<16 + 12>(w[2]); mmblk(2);
      waitvm_pin8x4<8 + 12>(w[3]); mmblk(3); issueV(1, 1);
      waitvm_pin8x4<16>(w[4]); mmblk(4);
      preK(0); preV(0);
    } else {
      issueK(0, 0); issueV(0, 0); issueK(1, 1); issueV(1, 1);
      asm volatile("s_barrier" ::: "memory");
      loadblk(0); loadblk(1); loadblk(2); loadblk(3); loadblk(4);
      preK(0); preV(0);
      waitvm<T_ + 16 + 40 + 8>();
      barrier();
      waitvm_pin8x4<32 + 8>(w[0]); mmblk(0);
      waitvm_pin8x4<24 + 8>(w[1]); mmblk(1);
      waitvm_pin8x4<16 + 8>(w[2]); mmblk(2);
      waitvm_pin8x4<8 + 8>(w[3]); mmblk(3);
      waitvm_pin8x4<8>(w[4]); mmblk(4);
    }
    asm volatile("" : "+v"(touch));
    barrier();
    if (l15 < 8) {
      const unsigned base = (lds0 + LDS_QP + (ig * 2 + nh) * 4096u + l15 * 512u + g * 128u) | ((l15 ^ (nh << 2)) << 4);
#pragma unroll
      for (int r = 0; r < 4; ++r) {
        lds_st<f32x4>(base ^ (32u * r), f32x4{qa[0][r], qa[1][r], qa[2][r], qa[3][r]});
        lds_st<f32x4>(base ^ (32u * r + 16u), f32x4{qa[4][r], qa[5][r], qa[6][r], qa[7][r]});
      }
    }
    preK(1);
    barrier();
    {
      const unsigned nh2 = lane >> 5, un = lane & 31, key = wave ^ (nh2 << 2);
      const unsigned row = lds0 + LDS_QP + nh2 * 4096u + wave * 512u;
      const unsigned ra = row + ((un ^ key) << 4);
      const f32x4 p0 = lds_ld<f32x4>(ra), p1 = lds_ld<f32x4>(ra + 8192u), p2 = lds_ld<f32x4>(ra + 16384u), p3 = lds_ld<f32x4>(ra + 24576u);
      const f32x4 sum = (p0 + p1) + (p2 + p3);
      lds_st<u32x2>(row + (((un & ~1u) ^ key) << 4) + (un & 1u) * 8u, u32x2{cvtpk(sum[0], sum[1]), cvtpk(sum[2], sum[3])});
    }
    barrier();
    const unsigned q = lane & 7, qh = q >> 2, m = bb * 4 + (q & 3);
    const unsigned rb = (lds0 + LDS_QP + qh * 4096u + m * 512u) | (((2 * g) ^ (m ^ (qh << 2))) << 4);
#pragma unroll
    for (int st = 0; st < 4; ++st) qf[st] = lds_ld<u32x4>(rb ^ (128u * st));
    preV(1);
    barrier();
  }

  f32x4 acc[8];
#pragma unroll
  for (int d = 0; d < 8; ++d) acc[d] = f32x4{0.f, 0.f, 0.f, 0.f};
  float mrun = -INFINITY, lrun = 0.f;
  const unsigned kro = l15 * 256u + ((g ^ l15) * 16u);
  const unsigned vro = g * 1024u + l15 * 16u;

#define TILE_BODY(KSLOT, VSLOT, WAITK, AFTERK, WAITV, AFTERV)                                                              \
  {                                                                                                                        \
    WAITK;                                                                                                                 \
    u32x4 kf[4];                                                                                                           \
    _Pragma("unroll") for (int st = 0; st < 4; ++st) kf[st] = lds_ld<u32x4>((KSLOT) + (kro ^ (st << 6)));                 \
    AFTERK;                                                                                                                \
    f32x4 s0 = {0.f, 0.f, 0.f, 0.f};                                                                                       \
    _Pragma("unroll") for (int st = 0; st < 4; ++st) s0 = mfma32(kf[st], qf[st], s0);                                      \
    float mx = xmax16_32(fmaxf(fmaxf(s0[0], s0[1]), fmaxf(s0[2], s0[3])));                                                 \
    const float mnew = fmaxf(mrun, mx);                                                                                    \
    const float alpha = __builtin_amdgcn_exp2f((mrun - mnew) * SC);                                                        \
    const float nm = -mnew * SC;                                                                                           \
    mrun = mnew;                                                                                                           \
    float p[4];                                                                                                            \
    _Pragma("unroll") for (int r = 0; r < 4; ++r) p[r] = __builtin_amdgcn_exp2f(fmaf(s0[r], SC, nm));                      \
    lrun = fmaf(lrun, alpha, (p[0] + p[1]) + (p[2] + p[3]));                                                               \
    const u32x2 pf = {cvtpk(p[0], p[1]), cvtpk(p[2], p[3])};                                                               \
    _Pragma("unroll") for (int d = 0; d < 8; ++d) acc[d] *= alpha;                                                         \
    asm volatile("" ::"v"(pf), "v"(acc[0]), "v"(acc[7]), "v"(lrun));     \
    WAITV;                                                                                                                 \
    u32x4 vr[4];                                                                                                           \
    _Pragma("unroll") for (int j = 0; j < 4; ++j) vr[j] = lds_ld<u32x4>((VSLOT) + vro + j * 256u);                         \
    AFTERV;                                                                                                                \
    _Pragma("unroll") for (int d = 0; d < 8; ++d) {                                                                        \
      const u32x2 a = {perm_sel(d & 1, vr[1][d >> 1], vr[0][d >> 1]), perm_sel(d & 1, vr[3][d >> 1], vr[2][d >> 1])};      \
      acc[d] = mfma16(a, pf, acc[d]);                                                                                      \
    }                                                                                                                      \
  }
#define PAIR_BODY(PAR, WAITK, AFTERK, WAITV, AFTERV) TILE_BODY(ring + (PAR) * 8192u, ring + (PAR) * 8192u + 4096u, WAITK, AFTERK, WAITV, AFTERV)
  constexpr int F = NPRE + 2;
  constexpr unsigned SH = F & 1;
  const unsigned stage = lds0 + LDS_QP + wave * 4096u;
#define STAGE(W, N) { waitvm_pin4<N>(W); waitlgkm0(); _Pragma("unroll") for (int i = 0; i < 4; ++i) lds_st<u32x4>(stage + i * 1024u + vv, W[i]); }
  static_assert(NPRE == 2, "wait counts of the first two tiles");
  PAIR_BODY(0u, waitvm<28>(), issueK(F, 0), waitvm<28>(), issueV(F, 0))
  PAIR_BODY(1u, waitvm<28>(), issueK(F + 1, 1), waitvm<28>(), issueV(F + 1, 1))
#define STAGED_TILE(t) TILE_BODY(stage, stage, STAGE(pk[t], 4 * (2 * NPRE - 1 - 2 * (t)) + 16), (void)0, STAGE(pv[t], 4 * (2 * NPRE - 2 - 2 * (t)) + 16), (void)0)
  STAGED_TILE(0)
  STAGED_TILE(1)
  PAIR_BODY(0u, waitvm<12>(), (void)0, waitvm<8>(), (void)0)
  PAIR_BODY(1u, waitvm<4>(), (void)0, { waitvm<0>(); issueK(F + 2, 0); }, (void)0)
  constexpr int N0 = F + 2;
  const bool fastx = rfl((unsigned)__builtin_amdgcn_s_getreg(20 | (3 << 11)) & 1u) == 0u;
#define THROTTLE() do { if (fastx) __builtin_amdgcn_s_sleep(5); } while (0)
#define STEADY(n, par) PAIR_BODY(par, { waitvm<0>(); THROTTLE(); issueV(n, par); }, (void)0, { waitvm<0>(); THROTTLE(); issueK((n) + 1, (par) ^ 1u); }, (void)0)
  for (int n = N0; n < WO_N0; ++n) {
    const unsigned par = (n + SH) & 1;
    STEADY(n, par)
  }
  u32x4 wo[9][4];
  const unsigned worot = bp & 7u;
  {
    const unsigned voff = g * (2u * 1152u) + l15 * 8u;
    const unsigned so = kvh * (256u * 1152u);
#pragma unroll
    for (int u = 0; u < 9; ++u) {
      const int n = WO_N0 + u;
      const unsigned par = (n + SH) & 1;
      const unsigned kb = u < 8 ? ((unsigned)u + worot) & 7u : wave;
      const unsigned uso = so + kb * (32u * 1152u) + (u < 8 ? wave * 128u : 1024u);
      u32x2 t[8];
      PAIR_BODY(par,
                { waitvm<0>(); THROTTLE(); _Pragma("unroll") for (int j = 0; j < 8; ++j) ld2(t[j], voff, srdWO, uso + (8 * (j & 3) + (j >> 2)) * 1152u); issueV(n, par); },
                (void)0,
                { waitvm_pin8<0>(t); THROTTLE(); issueK(n + 1, par ^ 1u);
                  _Pragma("unroll") for (int d = 0; d < 4; ++d) {
                    _Pragma("unroll") for (int i = 0; i < 4; ++i) wo[u][d][i] = perm_sel(d & 1, t[2 * i + 1][d >> 1], t[2 * i][d >> 1]);
                    asm volatile("" : "+v"(wo[u][d]));
                  } },
                (void)0)
    }
  }
  {
    const int ndeep = (bp & 1) ? NDEEP_B : NDEEP_A;
    static_assert(NDEEP_A >= 2 && NDEEP_B >= 2 && NDEEP_A <= TILES - WO_N0 - 9 && NDEEP_B <= TILES - WO_N0 - 9, "deep tail inside the last loop");
    int n = WO_N0 + 9;
    for (; n < TILES - ndeep; ++n) {
      const unsigned par = (n + SH) & 1;
      STEADY(n, par)
    }
    {
      const unsigned par = (n + SH) & 1;
      PAIR_BODY(par, { waitvm<0>(); issueV(n, par); }, issueK(n + 1, par ^ 1u), waitvm<4>(), issueV(n + 1, par ^ 1u))
    }
    for (++n; n < TILES - 1; ++n) {
      const unsigned par = (n + SH) & 1;
      PAIR_BODY(par, waitvm<4>(), { THROTTLE(); issueK(n + 1, par ^ 1u); }, waitvm<4>(), { THROTTLE(); issueV(n + 1, par ^ 1u); })
    }
    PAIR_BODY((TILES - 1 + SH) & 1u, waitvm<4>(), (void)0, waitvm<0>(), (void)0)
  }

  unsigned lane2;
  asm volatile("v_mbcnt_lo_u32_b32 %0, -1, 0\n\tv_mbcnt_hi_u32_b32 %0, -1, %0" : "=v"(lane2));
  const unsigned l15t = lane2 & 15, gt = lane2 >> 4, tidt = wave * 64u + lane2;
  const unsigned epoch_old = ld_agent((const unsigned*)(ws + WS_EPOCH + bp * 128u));
  lrun = xsum16_32(lrun);
  {
    const bool hi = l15t >= 8;
    const unsigned q = l15t & 7;
    const unsigned base = ((ring + q * 512u + gt * 128u) | (q << 4)) ^ (hi ? 64u : 0u);
#pragma unroll
    for (int d = 0; d < 4; ++d) lds_st<f32x4>(base ^ (d << 4), hi ? acc[4 + d] : acc[d]);
    if (lane2 < 8) lds_st<f32x2>(ring + 4096u + lane2 * 8u, f32x2{mrun, lrun});
  }
  barrier();
  {
    const unsigned q = (tidt >> 5) & 7, u = tidt & 31, w0 = lds0 + (tidt >> 8) * 65536u;
    f32x2 ml[4]; f32x4 pa[4];
#pragma unroll
    for (int w = 0; w < 4; ++w) ml[w] = lds_ld<f32x2>(w0 + w * 16384u + 4096u + q * 8u);
#pragma unroll
    for (int w = 0; w < 4; ++w) pa[w] = lds_ld<f32x4>(w0 + w * 16384u + q * 512u + ((u ^ q) * 16u));
    asm volatile("" : "+v"(pa[0]), "+v"(pa[1]), "+v"(pa[2]), "+v"(pa[3]), "+v"(ml[0]), "+v"(ml[1]), "+v"(ml[2]), "+v"(ml[3]));
    const float M = fmaxf(fmaxf(ml[0][0], ml[1][0]), fmaxf(ml[2][0], ml[3][0]));
    float e[4], L = 0.f;
#pragma unroll
    for (int w = 0; w < 4; ++w) { e[w] = __builtin_amdgcn_exp2f((ml[w][0] - M) * SC); L = fmaf(e[w], ml[w][1], L); }
    const float inv = __builtin_amdgcn_rcpf(L);
    const f32x4 o = ((e[0] * inv) * pa[0] + (e[1] * inv) * pa[1]) + ((e[2] * inv) * pa[2] + (e[3] * inv) * pa[3]);
    const unsigned m = (tidt >> 8) * 4 + (q & 3), un = (q >> 2) * 16 + (u >> 1);
    lds_st<u32x2>(lds0 + LDS_QP + m * 512u + ((un ^ m) * 16u) + (u & 1) * 8u, u32x2{cvtpk(o[0], o[1]), cvtpk(o[2], o[3])});
  }
  barrier();

  {
    const unsigned m = lane2 & 7;
    const unsigned ob = lds0 + LDS_QP + m * 512u;
    f32x4 oa[4], ra[4];
#pragma unroll
    for (int d = 0; d < 4; ++d) { oa[d] = f32x4{0.f, 0.f, 0.f, 0.f}; ra[d] = f32x4{0.f, 0.f, 0.f, 0.f}; }
    u32x4 bf[9];
#pragma unroll
    for (int kb = 0; kb < 9; ++kb) {
      const unsigned kk = kb < 8 ? ((unsigned)kb + worot) & 7u : wave;
      bf[kb] = lds_ld<u32x4>(ob + (((4 * kk + gt) ^ m) * 16u));
    }
    asm volatile("" : "+v"(bf[0]), "+v"(bf[1]), "+v"(bf[2]), "+v"(bf[3]), "+v"(bf[4]), "+v"(bf[5]), "+v"(bf[6]), "+v"(bf[7]), "+v"(bf[8]));
#pragma unroll
    for (int kb = 0; kb < 9; ++kb)
#pragma unroll
      for (int d = 0; d < 4; ++d) {
        if (kb < 8) oa[d] = mfma32(wo[kb][d], bf[kb], oa[d]); else ra[d] = mfma32(wo[kb][d], bf[kb], ra[d]);
      }
    {
      const bool hi = l15t >= 8;
      const unsigned dst = lds0 + (hi ? LDS_REM + (wave * 8 + m) * 272u : LDS_OUT + m * OUT_ROW + wave * 256u) + gt * 64u;
      f32x4 sel[4];
#pragma unroll
      for (int d = 0; d < 4; ++d) sel[d] = hi ? ra[d] : oa[d];
#pragma unroll
      for (int r = 0; r < 4; ++r) lds_st<f32x4>(dst + r * 16u, f32x4{sel[0][r], sel[1][r], sel[2][r], sel[3][r]});
    }
  }
  barrier();
  const __amdgpu_buffer_rsrc_t srdS = __builtin_amdgcn_make_buffer_rsrc((void*)(ws + WS_SLAB), (short)0, (int)SLAB_BYTES, 0x00020000);
  {
    const unsigned sl = (bp * HKV + kvh) * (8u * DM * 4u);
    constexpr int AUX = 16;
#pragma unroll
    for (int i = 0; i < 2; ++i) {
      const unsigned e = tidt + i * 512u, m = e >> 7, c = e & 127;
      __builtin_amdgcn_raw_buffer_store_b128(__builtin_bit_cast(u32x4, lds_ld<f32x4>(lds0 + LDS_OUT + m * OUT_ROW + c * 16u)), srdS, m * (DM * 4u) + c * 16u, sl, AUX);
    }
    if (tidt < 128) {
      const unsigned m = tidt >> 4, c = tidt & 15;
      f32x4 s = {0.f, 0.f, 0.f, 0.f};
#pragma unroll
      for (int w = 0; w < 8; ++w) s += lds_ld<f32x4>(lds0 + LDS_REM + (w * 8 + m) * 272u + c * 16u);
      __builtin_amdgcn_raw_buffer_store_b128(__builtin_bit_cast(u32x4, s), srdS, m * (DM * 4u) + 2048u + c * 16u, sl, AUX);
    }
  }
  waitvm<0>();
  barrier();
  if (wave >= 2) return;
  const unsigned epoch = epoch_old + TAG_STEP;
  unsigned* flags = (unsigned*)(ws + WS_FLAG + bp * 128u);
  if (tidt == 0) st_agent(flags + kvh, epoch);
  {
    const unsigned long long t0 = __builtin_amdgcn_s_memrealtime();
    bool ok;
    do {
      const unsigned f = ld_agent(flags + l15t);
      ok = __all(f == epoch);
      if (!ok) { __builtin_amdgcn_s_sleep(1); if (__builtin_amdgcn_s_memrealtime() - t0 > SPIN_LIMIT) break; }
    } while (!ok);
  }
  asm volatile("" ::: "memory");
  if (tidt < 72) {
    const unsigned m = kvh >> 1, c0 = (kvh & 1) * 288u + tidt * 4u;
    const unsigned off = m * (DM * 4u) + c0 * 4u;
    u32x4 v[HKV];
#pragma unroll
    for (int k = 0; k < HKV; ++k) v[k] = __builtin_amdgcn_raw_buffer_load_b128(srdS, off, (bp * HKV + k) * (8u * DM * 4u), 16);
    f32x4 s = __builtin_bit_cast(f32x4, v[0]);
#pragma unroll
    for (int k = 1; k < HKV; ++k) s += __builtin_bit_cast(f32x4, v[k]);
    const __amdgpu_buffer_rsrc_t srdO = __builtin_amdgcn_make_buffer_rsrc(outg, (short)0, NB * SQ * DM * 2, 0x00020000);
    __builtin_amdgcn_raw_buffer_store_b64(u32x2{cvtpk(s[0], s[1]), cvtpk(s[2], s[3])}, srdO, ((8 * bp + m) * DM + c0) * 2u, 0, 16);
  }
  if (tidt == 0 && kvh == 0) st_agent((unsigned*)(ws + WS_EPOCH + bp * 128u), epoch);
}

}

extern "C" void kernel_launch(void* const* d_in, const int* in_sizes, int n_in, void* d_out, int out_size, void* d_ws, size_t ws_size,
                              hipStream_t stream) {
  (void)in_sizes; (void)n_in; (void)out_size; (void)ws_size;
  (void)hipFuncSetAttribute((const void*)attn_fused_kernel, hipFuncAttributeMaxDynamicSharedMemorySize, LDS_BYTES);
  attn_fused_kernel<<<NWG, NTHR, LDS_BYTES, stream>>>(d_in[0], d_in[1], d_in[2], d_in[3], d_in[4], (char*)d_ws, d_out);
}
```
